```python
import torch

def _softmax(x, axis=-1):
    return torch.softmax(x, dim=axis)
def _gelu(x):
    return torch.nn.functional.gelu(x, approximate="tanh")
def _silu(x):
    return torch.nn.functional.silu(x)
def _sigmoid(x):
    return torch.sigmoid(x)
def _layernorm(x, axis=-1, eps=1e-5):
    m = x.mean(dim=axis, keepdim=True)
    v = x.var(dim=axis, keepdim=True, unbiased=False)
    return (x - m) / torch.sqrt(v + eps)
def _scatter_add(tgt, idx, src, axis=0):
    return tgt.clone().index_add_(axis, idx, src)
def _segment_sum(src, seg, n):
    out = torch.zeros((n,) + src.shape[1:], dtype=src.dtype)
    return out.index_add_(0, seg, src)
def _pad(x, pw):
    flat = tuple(v for pair in reversed(pw) for v in pair)
    return torch.nn.functional.pad(x, flat)

kernel_name = 'standalone__gqa_softmax__near_ridge'

def setup_inputs():

    _w = (lambda a: (lambda t: t.to(torch.bfloat16) if t.is_floating_point() else t)(torch.as_tensor(a)))
    B, Hq, Hkv, Sq, Sk, D = 8, 32, 8, 384, 512, 128
    return {
        "q": _w((lambda *s: torch.randn(*s, dtype=torch.bfloat16) * 0.8)(B, Hq, Sq, D)),
        "k": _w((lambda *s: torch.randn(*s, dtype=torch.bfloat16) * 0.8)(B, Hkv, Sk, D)),
        "v": _w((lambda *s: torch.randn(*s, dtype=torch.bfloat16) * 0.05)(B, Hkv, Sk, D)),
    }

def reference(q, k, v):
    B, Hq, Sq, D = q.shape
    Hkv = k.shape[1]
    rep = Hq // Hkv
    k = (lambda x, n, axis: torch.repeat_interleave(x, n, dim=axis))(k, rep, axis=1)
    v = (lambda x, n, axis: torch.repeat_interleave(x, n, dim=axis))(v, rep, axis=1)
    scale = D ** -0.5
    scores = torch.einsum("bhqd,bhkd->bhqk", q, k) * scale
    p = _softmax(scores, axis=-1)
    return torch.einsum("bhqk,bhkd->bhqd", p, v)
```

```cpp
#include <hip/hip_runtime.h>
#include <hip/hip_bf16.h>
constexpr int B=8,HQ=32,HKV=8,SQ=384,SK=512,D=128;
__device__ inline float bf2f(unsigned short x){ return __uint_as_float(((unsigned)x)<<16); }
__device__ inline unsigned short f2bf(float f){ unsigned u=__float_as_uint(f); u += 0x7fff + ((u>>16)&1); return (unsigned short)(u>>16); }
__global__ __launch_bounds__(256) void naive_kernel(const unsigned short* q,const unsigned short* k,const unsigned short* v,unsigned short* o){
  int row = blockIdx.x*4 + (threadIdx.x>>6); int lane = threadIdx.x&63;
  int bh = row / SQ; int b = bh / HQ, h = bh % HQ; int kvh = h / (HQ/HKV);
  const unsigned short* qr = q + (size_t)row*D;
  const unsigned short* kb = k + (size_t)(b*HKV+kvh)*SK*D;
  const unsigned short* vb = v + (size_t)(b*HKV+kvh)*SK*D;
  __shared__ float qs[4][D]; __shared__ float ps[4][SK];
  int w = threadIdx.x>>6;
  qs[w][lane]=bf2f(qr[lane]); qs[w][lane+64]=bf2f(qr[lane+64]);
  __syncthreads();
  float s[8]; float m=-1e30f;
  for(int i=0;i<8;i++){ int key=i*64+lane; float a=0; for(int d=0;d<D;d++) a+=qs[w][d]*bf2f(kb[key*D+d]); s[i]=a*0.08838834764831845f; m=fmaxf(m,s[i]); }
  for(int o2=32;o2>0;o2>>=1) m=fmaxf(m,__shfl_xor(m,o2));
  float l=0; for(int i=0;i<8;i++){ s[i]=__expf(s[i]-m); l+=s[i]; ps[w][i*64+lane]=s[i]; }
  for(int o2=32;o2>0;o2>>=1) l+=__shfl_xor(l,o2);
  __syncthreads();
  float a0=0,a1=0; for(int key=0;key<SK;key++){ float p=ps[w][key]; a0+=p*bf2f(vb[key*D+lane]); a1+=p*bf2f(vb[key*D+lane+64]); }
  o[(size_t)row*D+lane]=f2bf(a0/l); o[(size_t)row*D+lane+64]=f2bf(a1/l);
}
extern "C" void kernel_launch(void* const* d_in, const int* in_sizes, int n_in, void* d_out, int out_size, void* d_ws, size_t ws_size, hipStream_t stream) {
  naive_kernel<<<B*HQ*SQ/4,256,0,stream>>>((const unsigned short*)d_in[0],(const unsigned short*)d_in[1],(const unsigned short*)d_in[2],(unsigned short*)d_out);
}
```

```cpp
#include <hip/hip_runtime.h>
#include <stdint.h>

typedef unsigned short u16;
typedef unsigned int u32;
typedef u32 u32x4 __attribute__((ext_vector_type(4)));

__device__ __forceinline__ float bf_lo(u32 x) { return __uint_as_float(x << 16); }
__device__ __forceinline__ float bf_hi(u32 x) { return __uint_as_float(x & 0xffff0000u); }
__device__ __forceinline__ u16 f2bf(float f) {
  u32 u = __float_as_uint(f);
  if ((u & 0x7fffffffu) > 0x7f800000u) return (u16)((u >> 16) | 0x40u);
  u += 0x7fffu + ((u >> 16) & 1u);
  return (u16)(u >> 16);
}

extern "C" __global__ __launch_bounds__(256, 1) void standalone__gqa_softmax__near_ridge_kernel(const u16* __restrict__ q, const u16* __restrict__ k, const u16* __restrict__ v,
                                                           u16* __restrict__ o) {
  __shared__ __attribute__((aligned(16))) char lds[163840];
  const u32 bid = blockIdx.x;
  const u32 wid = __builtin_amdgcn_readfirstlane(threadIdx.x >> 6);
  const u32 grp = (bid & 7u) * 8u + (bid >> 5), head = grp * 4u + ((bid >> 3) & 3u);
  const uint64_t qb = (uint64_t)q + (uint64_t)head * 98304u, ob = (uint64_t)o + (uint64_t)head * 98304u;
  const uint64_t kb = (uint64_t)k + (uint64_t)grp * 131072u, vb = (uint64_t)v + (uint64_t)grp * 131072u;
  const u32 hq = (bid >> 3) & 3u;

  u32 bad;
  asm volatile(
      "s_mov_b64 s[40:41], %[kb]\n\t"
      "s_and_b32 s41, s41, 0xffff\n\t"
      "s_mov_b32 s42, 131072\n\t"
      "s_mov_b32 s43, 0x20000\n\t"
      "s_mov_b32 s52, %[wid]\n\t"
      "s_mov_b32 s59, 0\n\t"
      "v_mbcnt_lo_u32_b32 v238, -1, 0\n\t"
      "v_mbcnt_hi_u32_b32 v238, -1, v238\n\t"
      "v_lshrrev_b32 v244, 4, v238\n\t"
      "v_and_b32 v243, 15, v238\n\t"
      "v_xor_b32 v241, v243, v244\n\t"
      "v_lshlrev_b32 v241, 4, v241\n\t"
      "v_lshlrev_b32 v242, 8, v244\n\t"
      "v_add_u32 v226, v241, v242\n\t"
      "v_xor_b32 v229, 64, v226\n\t"
      "s_add_u32 s64, s59, 0\n\t"
      "s_and_b32 s64, s64, 3\n\t"
      "s_lshl_b32 s72, s64, 13\n\t"
      "s_add_u32 s64, s59, 1\n\t"
      "s_and_b32 s64, s64, 3\n\t"
      "s_lshl_b32 s73, s64, 13\n\t"
      "s_add_u32 s64, s59, 2\n\t"
      "s_and_b32 s64, s64, 3\n\t"
      "s_lshl_b32 s74, s64, 13\n\t"
      "s_add_u32 s64, s59, 3\n\t"
      "s_and_b32 s64, s64, 3\n\t"
      "s_lshl_b32 s75, s64, 13\n\t"
      "s_add_u32 s63, s52, s59\n\t"
      "s_and_b32 s64, s63, 3\n\t"
      "s_lshl_b32 s66, s64, 15\n\t"
      "s_lshl_b32 s65, s52, 13\n\t"
      "s_add_u32 m0, s65, 0\n\t"
      "s_add_u32 s64, s66, s72\n\t"
      "buffer_load_dwordx4 v226, s[40:43], s64 offen lds\n\t"
      "buffer_load_dwordx4 v229, s[40:43], s64 offen offset:1024 lds\n\t"
      "buffer_load_dwordx4 v226, s[40:43], s64 offen offset:2048 lds\n\t"
      "buffer_load_dwordx4 v229, s[40:43], s64 offen offset:3072 lds\n\t"
      "s_add_u32 m0, s65, 4096\n\t"
      "s_add_u32 s64, s66, s72\n\t"
      "s_add_u32 s64, s64, 4096\n\t"
      "buffer_load_dwordx4 v226, s[40:43], s64 offen lds\n\t"
      "buffer_load_dwordx4 v229, s[40:43], s64 offen offset:1024 lds\n\t"
      "buffer_load_dwordx4 v226, s[40:43], s64 offen offset:2048 lds\n\t"
      "buffer_load_dwordx4 v229, s[40:43], s64 offen offset:3072 lds\n\t"
      "s_mov_b64 s[36:37], %[qb]\n\t"
      "s_and_b32 s37, s37, 0xffff\n\t"
      "s_mov_b32 s38, 98304\n\t"
      "s_mov_b32 s39, 0x20000\n\t"
      "s_mul_i32 s56, s52, 2176\n\t"
      "s_add_u32 s56, s56, 131072\n\t"
      "s_add_u32 s57, s56, 64\n\t"
      "s_lshl_b32 s54, s52, 11\n\t"
      "s_mov_b32 s55, 0\n\t"
      "s_add_u32 m0, s55, s56\n\t"
      "s_nop 0\n\t"
      "buffer_load_dwordx4 v226, s[36:39], s54 offen nt lds\n\t"
      "s_add_u32 m0, s55, s57\n\t"
      "s_nop 0\n\t"
      "buffer_load_dwordx4 v226, s[36:39], s54 offen offset:1024 nt lds\n\t"
      "s_add_u32 s55, s55, 8704\n\t"
      "s_cmp_eq_u32 s55, 26112\n\t"
      "s_cselect_b32 s55, 0, s55\n\t"
      "s_add_u32 s54, s54, 8192\n\t"
      "s_mov_b64 s[44:45], %[vb]\n\t"
      "s_and_b32 s45, s45, 0xffff\n\t"
      "s_mov_b32 s46, 131072\n\t"
      "s_mov_b32 s47, 0x20000\n\t"
      "s_mov_b64 s[48:49], %[ob]\n\t"
      "s_and_b32 s49, s49, 0xffff\n\t"
      "s_mov_b32 s50, 0\n\t"
      "s_mov_b32 s51, 0x20000\n\t"
      "v_lshrrev_b32 v239, 2, v243\n\t"
      "v_mul_u32_u24 v239, 1088, v239\n\t"
      "v_and_b32 v240, 3, v243\n\t"
      "v_lshlrev_b32 v241, 8, v240\n\t"
      "v_add_u32 v239, v239, v241\n\t"
      "v_xor_b32 v241, v244, v240\n\t"
      "v_lshlrev_b32 v241, 4, v241\n\t"
      "v_add_u32 v239, v239, v241\n\t"
      "v_add_u32 v217, 131072, v239\n\t"
      "v_mov_b32 v216, v217\n\t"
      "s_add_u32 m0, s65, 32768\n\t"
      "s_add_u32 s64, s66, s73\n\t"
      "buffer_load_dwordx4 v226, s[40:43], s64 offen lds\n\t"
      "v_lshlrev_b32 v239, 8, v243\n\t"
      "v_add_u32 v239, s65, v239\n\t"
      "v_and_b32 v240, 7, v243\n\t"
      "v_xor_b32 v241, v244, v240\n\t"
      "v_lshlrev_b32 v242, 4, v241\n\t"
      "v_add_u32 v236, v239, v242\n\t"
      "v_xor_b32 v241, 4, v241\n\t"
      "v_lshlrev_b32 v242, 4, v241\n\t"
      "v_add_u32 v237, v239, v242\n\t"
      "v_lshlrev_b32 v239, 4, v238\n\t"
      "v_add_u32 v218, s65, v239\n\t"
      "s_add_u32 s62, s52, 1\n\t"
      "s_and_b32 s62, s62, 3\n\t"
      "s_lshl_b32 s62, s62, 13\n\t"
      "v_add_u32 v219, s62, v239\n\t"
      "s_add_u32 s62, s52, 2\n\t"
      "s_and_b32 s62, s62, 3\n\t"
      "s_lshl_b32 s62, s62, 13\n\t"
      "buffer_load_dwordx4 v229, s[40:43], s64 offen offset:1024 lds\n\t"
      "v_add_u32 v220, s62, v239\n\t"
      "s_add_u32 s62, s52, 3\n\t"
      "s_and_b32 s62, s62, 3\n\t"
      "s_lshl_b32 s62, s62, 13\n\t"
      "v_add_u32 v221, s62, v239\n\t"
      "v_mul_u32_u24 v239, 272, v243\n\t"
      "v_lshlrev_b32 v240, 3, v244\n\t"
      "v_add_u32 v239, v239, v240\n\t"
      "s_lshl_b32 s62, s52, 6\n\t"
      "s_add_u32 s63, s62, 98304\n\t"
      "v_add_u32 v222, s63, v239\n\t"
      "v_lshlrev_b32 v239, 1, v244\n\t"
      "v_xor_b32 v239, v243, v239\n\t"
      "v_lshlrev_b32 v239, 4, v239\n\t"
      "v_lshlrev_b32 v240, 8, v244\n\t"
      "v_add_u32 v230, v239, v240\n\t"
      "v_xor_b32 v231, 128, v230\n\t"
      "v_lshrrev_b32 v239, 2, v238\n\t"
      "buffer_load_dwordx4 v226, s[40:43], s64 offen offset:2048 lds\n\t"
      "v_and_b32 v240, 7, v239\n\t"
      "v_lshlrev_b32 v240, 1, v240\n\t"
      "s_lshl_b32 s62, s52, 2\n\t"
      "v_xor_b32 v240, s62, v240\n\t"
      "v_lshlrev_b32 v239, 8, v239\n\t"
      "v_and_b32 v241, 3, v238\n\t"
      "v_lshlrev_b32 v241, 3, v241\n\t"
      "v_add_u32 v239, v239, v241\n\t"
      "v_lshlrev_b32 v241, 4, v240\n\t"
      "v_add_u32 v241, v241, v239\n\t"
      "v_xor_b32 v240, 2, v240\n\t"
      "v_lshlrev_b32 v242, 4, v240\n\t"
      "v_add_u32 v242, v242, v239\n\t"
      "s_add_u32 s62, s52, 0\n\t"
      "s_and_b32 s62, s62, 3\n\t"
      "s_lshl_b32 s62, s62, 13\n\t"
      "s_add_u32 s62, s62, 32768\n\t"
      "v_add_u32 v246, s62, v241\n\t"
      "buffer_load_dwordx4 v229, s[40:43], s64 offen offset:3072 lds\n\t"
      "v_add_u32 v247, s62, v242\n\t"
      "s_add_u32 s62, s52, 1\n\t"
      "s_and_b32 s62, s62, 3\n\t"
      "s_lshl_b32 s62, s62, 13\n\t"
      "s_add_u32 s62, s62, 32768\n\t"
      "v_add_u32 v248, s62, v241\n\t"
      "v_add_u32 v249, s62, v242\n\t"
      "s_add_u32 s62, s52, 2\n\t"
      "s_and_b32 s62, s62, 3\n\t"
      "s_lshl_b32 s62, s62, 13\n\t"
      "s_add_u32 s62, s62, 32768\n\t"
      "v_add_u32 v250, s62, v241\n\t"
      "v_add_u32 v251, s62, v242\n\t"
      "s_add_u32 s62, s52, 3\n\t"
      "s_and_b32 s62, s62, 3\n\t"
      "s_lshl_b32 s62, s62, 13\n\t"
      "s_add_u32 s62, s62, 32768\n\t"
      "v_add_u32 v252, s62, v241\n\t"
      "s_add_u32 m0, s65, 36864\n\t"
      "s_add_u32 s64, s66, s73\n\t"
      "s_add_u32 s64, s64, 4096\n\t"
      "buffer_load_dwordx4 v226, s[40:43], s64 offen lds\n\t"
      "v_add_u32 v253, s62, v242\n\t"
      "s_lshl_b32 s62, s52, 3\n\t"
      "v_add_u32 v239, s62, v244\n\t"
      "v_mul_u32_u24 v240, 272, v239\n\t"
      "v_lshlrev_b32 v241, 4, v243\n\t"
      "v_add_u32 v240, v240, v241\n\t"
      "v_add_u32 v223, 98304, v240\n\t"
      "v_lshlrev_b32 v240, 8, v239\n\t"
      "v_add_u32 v227, v240, v241\n\t"
      "v_and_b32 v239, 31, v238\n\t"
      "v_lshlrev_b32 v239, 4, v239\n\t"
      "v_add_u32 v225, 157696, v239\n\t"
      "s_lshl_b32 s62, s52, 2\n\t"
      "v_add_u32 v224, s62, v225\n\t"
      "v_and_b32 v239, 3, v238\n\t"
      "v_cmp_gt_u32 vcc, 2, v239\n\t"
      "v_mov_b32 v241, 0x3f803f80\n\t"
      "s_nop 0\n\t"
      "v_cndmask_b32 v192, 0, v241, vcc\n\t"
      "buffer_load_dwordx4 v229, s[40:43], s64 offen offset:1024 lds\n\t"
      "v_mov_b32 v193, v192\n\t"
      "v_mov_b32 v194, v192\n\t"
      "v_mov_b32 v195, v192\n\t"
      "v_cmp_eq_u32 vcc, 0, v239\n\t"
      "v_mov_b32 v241, 0xeeeeeeee\n\t"
      "v_mov_b32 v242, 0x44444444\n\t"
      "s_nop 0\n\t"
      "v_cndmask_b32 v196, v241, v242, vcc\n\t"
      "v_mov_b32 v184, 0\n\t"
      "v_mov_b32 v185, 0\n\t"
      "v_mov_b32 v186, 0\n\t"
      "v_mov_b32 v187, 0\n\t"
      "v_mov_b32 v188, 0\n\t"
      "v_mov_b32 v189, 0\n\t"
      "v_mov_b32 v190, 0\n\t"
      "v_mov_b32 v191, 0\n\t"
      "v_mov_b32 v200, 0\n\t"
      "v_mov_b32 v203, 158720\n\t"
      "buffer_load_dwordx4 v226, s[40:43], s64 offen offset:2048 lds\n\t"
      "v_mov_b32 v239, 0\n\t"
      "ds_write_b32 v203, v239\n\t"
      "s_mov_b32 s71, 0\n\t"
      "s_mov_b32 s60, 0xffff0000\n\t"
      "s_mov_b32 s61, 0xffff0000\n\t"
      "s_mov_b32 s58, 0xffffe000\n\t"
      "s_mov_b32 s53, 0\n\t"
      "v_and_b32 v239, 3, v238\n\t"
      "v_lshrrev_b32 v240, 3, v238\n\t"
      "v_and_b32 v240, 1, v240\n\t"
      "v_lshrrev_b32 v241, 1, v239\n\t"
      "v_lshlrev_b32 v242, 1, v240\n\t"
      "v_add_u32 v241, v241, v242\n\t"
      "v_and_b32 v239, 1, v238\n\t"
      "v_lshlrev_b32 v239, 4, v239\n\t"
      "v_mov_b32 v240, 0x3e03\n\t"
      "v_mov_b32 v242, 0xb9d8\n\t"
      "v_lshlrev_b32 v240, v239, v240\n\t"
      "buffer_load_dwordx4 v229, s[40:43], s64 offen offset:3072 lds\n\t"
      "v_lshlrev_b32 v242, v239, v242\n\t"
      "v_lshrrev_b32 v239, 3, v238\n\t"
      "v_xor_b32 v239, v239, v238\n\t"
      "v_and_b32 v239, 4, v239\n\t"
      "v_and_b32 v245, 16, v238\n\t"
      "v_or_b32 v239, v239, v245\n\t"
      "v_add_u32 v241, v241, v239\n\t"
      "v_cmp_eq_u32 vcc, 0, v241\n\t"
      "s_nop 1\n\t"
      "v_cndmask_b32 v208, 0, v240, vcc\n\t"
      "v_cndmask_b32 v212, 0, v242, vcc\n\t"
      "v_cmp_eq_u32 vcc, 1, v241\n\t"
      "s_nop 1\n\t"
      "v_cndmask_b32 v209, 0, v240, vcc\n\t"
      "v_cndmask_b32 v213, 0, v242, vcc\n\t"
      "v_cmp_eq_u32 vcc, 2, v241\n\t"
      "s_nop 1\n\t"
      "v_cndmask_b32 v210, 0, v240, vcc\n\t"
      "v_cndmask_b32 v214, 0, v242, vcc\n\t"
      "v_cmp_eq_u32 vcc, 3, v241\n\t"
      "s_nop 1\n\t"
      "v_cndmask_b32 v211, 0, v240, vcc\n\t"
      "v_cndmask_b32 v215, 0, v242, vcc\n\t"
      "s_add_u32 m0, s65, 65536\n\t"
      "s_add_u32 s64, s66, s74\n\t"
      "buffer_load_dwordx4 v226, s[40:43], s64 offen lds\n\t"
      "buffer_load_dwordx4 v229, s[40:43], s64 offen offset:1024 lds\n\t"
      "s_waitcnt vmcnt(12)\n\t"
      "ds_read_b128 v[112:115], v236 offset:0\n\t"
      "ds_read_b128 v[116:119], v237 offset:0\n\t"
      "ds_read_b128 v[120:123], v236 offset:128\n\t"
      "ds_read_b128 v[124:127], v237 offset:128\n\t"
      "ds_read_b128 v[128:131], v236 offset:4096\n\t"
      "ds_read_b128 v[132:135], v237 offset:4096\n\t"
      "ds_read_b128 v[136:139], v236 offset:4224\n\t"
      "ds_read_b128 v[140:143], v237 offset:4224\n\t"
      "s_waitcnt vmcnt(10) lgkmcnt(0)\n\t"
      "s_barrier\n\t"
      "ds_read_b128 v[16:19], v216 offset:0\n\t"
      "ds_read_b128 v[20:23], v216 offset:64\n\t"
      "ds_read_b128 v[24:27], v216 offset:128\n\t"
      "ds_read_b128 v[28:31], v216 offset:192\n\t"
      "ds_read_b128 v[32:35], v216 offset:4352\n\t"
      "ds_read_b128 v[36:39], v216 offset:4416\n\t"
      "ds_read_b128 v[40:43], v216 offset:4480\n\t"
      "ds_read_b128 v[44:47], v216 offset:4544\n\t"
      "v_mfma_f32_32x32x16_bf16 v[144:159], v[208:211], v[112:115], 0\n\t"
      "v_mfma_f32_32x32x16_bf16 v[144:159], v[212:215], v[112:115], v[144:159]\n\t"
      "buffer_load_dwordx4 v226, s[40:43], s64 offen offset:2048 lds\n\t"
      "v_mfma_f32_32x32x16_bf16 v[160:175], v[208:211], v[116:119], 0\n\t"
      "buffer_load_dwordx4 v229, s[40:43], s64 offen offset:3072 lds\n\t"
      "s_add_u32 m0, s65, 69632\n\t"
      "s_add_u32 s64, s66, s74\n\t"
      "s_add_u32 s64, s64, 4096\n\t"
      "v_mfma_f32_32x32x16_bf16 v[160:175], v[212:215], v[116:119], v[160:175]\n\t"
      "buffer_load_dwordx4 v226, s[40:43], s64 offen lds\n\t"
      "v_cvt_pk_bf16_f32 v152, v144, v145\n\t"
      "v_cvt_pk_bf16_f32 v153, v146, v147\n\t"
      "v_cvt_pk_bf16_f32 v154, v148, v149\n\t"
      "v_cvt_pk_bf16_f32 v155, v150, v151\n\t"
      "v_mfma_f32_32x32x16_bf16 v[0:15], v[208:211], v[120:123], 0\n\t"
      "buffer_load_dwordx4 v229, s[40:43], s64 offen offset:1024 lds\n\t"
      "v_accvgpr_write_b32 a0, v152\n\t"
      "v_accvgpr_write_b32 a1, v153\n\t"
      "v_accvgpr_write_b32 a2, v154\n\t"
      "v_accvgpr_write_b32 a3, v155\n\t"
      "v_mfma_f32_32x32x16_bf16 v[0:15], v[212:215], v[120:123], v[0:15]\n\t"
      "buffer_load_dwordx4 v226, s[40:43], s64 offen offset:2048 lds\n\t"
      "v_cvt_pk_bf16_f32 v168, v160, v161\n\t"
      "v_cvt_pk_bf16_f32 v169, v162, v163\n\t"
      "v_cvt_pk_bf16_f32 v170, v164, v165\n\t"
      "v_cvt_pk_bf16_f32 v171, v166, v167\n\t"
      "v_mfma_f32_32x32x16_bf16 v[144:159], v[208:211], v[124:127], 0\n\t"
      "buffer_load_dwordx4 v229, s[40:43], s64 offen offset:3072 lds\n\t"
      "s_add_u32 m0, s65, 98304\n\t"
      "s_add_u32 s64, s66, s75\n\t"
      "v_accvgpr_write_b32 a4, v168\n\t"
      "v_accvgpr_write_b32 a5, v169\n\t"
      "v_accvgpr_write_b32 a6, v170\n\t"
      "v_accvgpr_write_b32 a7, v171\n\t"
      "v_mfma_f32_32x32x16_bf16 v[144:159], v[212:215], v[124:127], v[144:159]\n\t"
      "buffer_load_dwordx4 v226, s[40:43], s64 offen lds\n\t"
      "v_cvt_pk_bf16_f32 v8, v0, v1\n\t"
      "v_cvt_pk_bf16_f32 v9, v2, v3\n\t"
      "v_cvt_pk_bf16_f32 v10, v4, v5\n\t"
      "v_cvt_pk_bf16_f32 v11, v6, v7\n\t"
      "v_mfma_f32_32x32x16_bf16 v[160:175], v[208:211], v[128:131], 0\n\t"
      "v_accvgpr_write_b32 a8, v8\n\t"
      "v_accvgpr_write_b32 a9, v9\n\t"
      "v_accvgpr_write_b32 a10, v10\n\t"
      "v_accvgpr_write_b32 a11, v11\n\t"
      "v_mfma_f32_32x32x16_bf16 v[160:175], v[212:215], v[128:131], v[160:175]\n\t"
      "buffer_load_dwordx4 v229, s[40:43], s64 offen offset:1024 lds\n\t"
      "v_cvt_pk_bf16_f32 v152, v144, v145\n\t"
      "v_cvt_pk_bf16_f32 v153, v146, v147\n\t"
      "v_cvt_pk_bf16_f32 v154, v148, v149\n\t"
      "v_cvt_pk_bf16_f32 v155, v150, v151\n\t"
      "v_mfma_f32_32x32x16_bf16 v[0:15], v[208:211], v[132:135], 0\n\t"
      "v_accvgpr_write_b32 a12, v152\n\t"
      "v_accvgpr_write_b32 a13, v153\n\t"
      "v_accvgpr_write_b32 a14, v154\n\t"
      "v_accvgpr_write_b32 a15, v155\n\t"
      "v_mfma_f32_32x32x16_bf16 v[0:15], v[212:215], v[132:135], v[0:15]\n\t"
      "buffer_load_dwordx4 v226, s[40:43], s64 offen offset:2048 lds\n\t"
      "v_cvt_pk_bf16_f32 v168, v160, v161\n\t"
      "v_cvt_pk_bf16_f32 v169, v162, v163\n\t"
      "v_cvt_pk_bf16_f32 v170, v164, v165\n\t"
      "v_cvt_pk_bf16_f32 v171, v166, v167\n\t"
      "v_mfma_f32_32x32x16_bf16 v[144:159], v[208:211], v[136:139], 0\n\t"
      "v_accvgpr_write_b32 a16, v168\n\t"
      "v_accvgpr_write_b32 a17, v169\n\t"
      "v_accvgpr_write_b32 a18, v170\n\t"
      "v_accvgpr_write_b32 a19, v171\n\t"
      "v_mfma_f32_32x32x16_bf16 v[144:159], v[212:215], v[136:139], v[144:159]\n\t"
      "buffer_load_dwordx4 v229, s[40:43], s64 offen offset:3072 lds\n\t"
      "v_cvt_pk_bf16_f32 v8, v0, v1\n\t"
      "v_cvt_pk_bf16_f32 v9, v2, v3\n\t"
      "v_cvt_pk_bf16_f32 v10, v4, v5\n\t"
      "v_cvt_pk_bf16_f32 v11, v6, v7\n\t"
      "v_mfma_f32_32x32x16_bf16 v[160:175], v[208:211], v[140:143], 0\n\t"
      "s_add_u32 m0, s65, 102400\n\t"
      "s_add_u32 s64, s66, s75\n\t"
      "s_add_u32 s64, s64, 4096\n\t"
      "v_accvgpr_write_b32 a20, v8\n\t"
      "v_accvgpr_write_b32 a21, v9\n\t"
      "v_accvgpr_write_b32 a22, v10\n\t"
      "v_accvgpr_write_b32 a23, v11\n\t"
      "v_mfma_f32_32x32x16_bf16 v[160:175], v[212:215], v[140:143], v[160:175]\n\t"
      "buffer_load_dwordx4 v226, s[40:43], s64 offen lds\n\t"
      "v_cvt_pk_bf16_f32 v152, v144, v145\n\t"
      "v_cvt_pk_bf16_f32 v153, v146, v147\n\t"
      "v_cvt_pk_bf16_f32 v154, v148, v149\n\t"
      "v_cvt_pk_bf16_f32 v155, v150, v151\n\t"
      "s_waitcnt lgkmcnt(0)\n\t"
      "v_mfma_f32_16x16x32_bf16 v[48:51], a[0:3], v[16:19], 0\n\t"
      "v_accvgpr_write_b32 a24, v152\n\t"
      "v_accvgpr_write_b32 a25, v153\n\t"
      "v_accvgpr_write_b32 a26, v154\n\t"
      "v_accvgpr_write_b32 a27, v155\n\t"
      "v_mfma_f32_16x16x32_bf16 v[48:51], a[4:7], v[20:23], v[48:51]\n\t"
      "s_waitcnt vmcnt(13)\n\t"
      "ds_read_b128 v[112:115], v236 offset:32768\n\t"
      "ds_read_b128 v[116:119], v237 offset:32768\n\t"
      "ds_read_b128 v[120:123], v236 offset:32896\n\t"
      "ds_read_b128 v[124:127], v237 offset:32896\n\t"
      "buffer_load_dwordx4 v229, s[40:43], s64 offen offset:1024 lds\n\t"
      "v_cvt_pk_bf16_f32 v168, v160, v161\n\t"
      "v_cvt_pk_bf16_f32 v169, v162, v163\n\t"
      "v_cvt_pk_bf16_f32 v170, v164, v165\n\t"
      "v_cvt_pk_bf16_f32 v171, v166, v167\n\t"
      "v_mfma_f32_16x16x32_bf16 v[48:51], a[8:11], v[24:27], v[48:51]\n\t"
      "ds_read_b128 v[128:131], v236 offset:36864\n\t"
      "ds_read_b128 v[132:135], v237 offset:36864\n\t"
      "ds_read_b128 v[136:139], v236 offset:36992\n\t"
      "ds_read_b128 v[140:143], v237 offset:36992\n\t"
      "v_accvgpr_write_b32 a28, v168\n\t"
      "v_accvgpr_write_b32 a29, v169\n\t"
      "v_accvgpr_write_b32 a30, v170\n\t"
      "v_accvgpr_write_b32 a31, v171\n\t"
      "v_mfma_f32_16x16x32_bf16 v[48:51], a[12:15], v[28:31], v[48:51]\n\t"
      "buffer_load_dwordx4 v226, s[40:43], s64 offen offset:2048 lds\n\t"
      "v_mfma_f32_16x16x32_bf16 v[52:55], a[16:19], v[16:19], 0\n\t"
      "v_mfma_f32_16x16x32_bf16 v[52:55], a[20:23], v[20:23], v[52:55]\n\t"
      "buffer_load_dwordx4 v229, s[40:43], s64 offen offset:3072 lds\n\t"
      "v_mfma_f32_16x16x32_bf16 v[52:55], a[24:27], v[24:27], v[52:55]\n\t"
      "v_mfma_f32_16x16x32_bf16 v[52:55], a[28:31], v[28:31], v[52:55]\n\t"
      "v_mfma_f32_16x16x32_bf16 v[56:59], a[0:3], v[32:35], 0\n\t"
      "v_mfma_f32_16x16x32_bf16 v[56:59], a[4:7], v[36:39], v[56:59]\n\t"
      "v_mfma_f32_16x16x32_bf16 v[56:59], a[8:11], v[40:43], v[56:59]\n\t"
      "v_mfma_f32_16x16x32_bf16 v[56:59], a[12:15], v[44:47], v[56:59]\n\t"
      "v_mfma_f32_16x16x32_bf16 v[60:63], a[16:19], v[32:35], 0\n\t"
      "v_mfma_f32_16x16x32_bf16 v[60:63], a[20:23], v[36:39], v[60:63]\n\t"
      "v_mfma_f32_16x16x32_bf16 v[60:63], a[24:27], v[40:43], v[60:63]\n\t"
      "v_mfma_f32_16x16x32_bf16 v[60:63], a[28:31], v[44:47], v[60:63]\n\t"
      "s_waitcnt lgkmcnt(0)\n\t"
      "v_mfma_f32_32x32x16_bf16 v[144:159], v[208:211], v[112:115], 0\n\t"
      "s_add_u32 m0, s55, s56\n\t"
      "v_exp_f32 v48, v48\n\t"
      "v_mfma_f32_32x32x16_bf16 v[144:159], v[212:215], v[112:115], v[144:159]\n\t"
      "buffer_load_dwordx4 v226, s[36:39], s54 offen nt lds\n\t"
      "v_exp_f32 v49, v49\n\t"
      "v_mfma_f32_32x32x16_bf16 v[160:175], v[208:211], v[116:119], 0\n\t"
      "v_exp_f32 v50, v50\n\t"
      "v_mfma_f32_32x32x16_bf16 v[160:175], v[212:215], v[116:119], v[160:175]\n\t"
      "v_cvt_pk_bf16_f32 v152, v144, v145\n\t"
      "v_cvt_pk_bf16_f32 v153, v146, v147\n\t"
      "v_cvt_pk_bf16_f32 v154, v148, v149\n\t"
      "v_cvt_pk_bf16_f32 v155, v150, v151\n\t"
      "v_exp_f32 v51, v51\n\t"
      "v_mfma_f32_32x32x16_bf16 v[0:15], v[208:211], v[120:123], 0\n\t"
      "s_add_u32 m0, s55, s57\n\t"
      "v_accvgpr_write_b32 a32, v152\n\t"
      "v_accvgpr_write_b32 a33, v153\n\t"
      "v_accvgpr_write_b32 a34, v154\n\t"
      "v_accvgpr_write_b32 a35, v155\n\t"
      "v_exp_f32 v52, v52\n\t"
      "v_mfma_f32_32x32x16_bf16 v[0:15], v[212:215], v[120:123], v[0:15]\n\t"
      "buffer_load_dwordx4 v226, s[36:39], s54 offen offset:1024 nt lds\n\t"
      "s_add_u32 s55, s55, 8704\n\t"
      "s_cmp_eq_u32 s55, 26112\n\t"
      "s_cselect_b32 s55, 0, s55\n\t"
      "s_add_u32 s54, s54, 8192\n\t"
      "v_cvt_pk_bf16_f32 v168, v160, v161\n\t"
      "v_cvt_pk_bf16_f32 v169, v162, v163\n\t"
      "v_cvt_pk_bf16_f32 v170, v164, v165\n\t"
      "v_cvt_pk_bf16_f32 v171, v166, v167\n\t"
      "v_exp_f32 v53, v53\n\t"
      "v_mfma_f32_32x32x16_bf16 v[144:159], v[208:211], v[124:127], 0\n\t"
      "v_accvgpr_write_b32 a36, v168\n\t"
      "v_accvgpr_write_b32 a37, v169\n\t"
      "v_accvgpr_write_b32 a38, v170\n\t"
      "v_accvgpr_write_b32 a39, v171\n\t"
      "v_exp_f32 v54, v54\n\t"
      "v_mfma_f32_32x32x16_bf16 v[144:159], v[212:215], v[124:127], v[144:159]\n\t"
      "v_cvt_pk_bf16_f32 v8, v0, v1\n\t"
      "v_cvt_pk_bf16_f32 v9, v2, v3\n\t"
      "v_cvt_pk_bf16_f32 v10, v4, v5\n\t"
      "v_cvt_pk_bf16_f32 v11, v6, v7\n\t"
      "v_exp_f32 v55, v55\n\t"
      "v_mfma_f32_32x32x16_bf16 v[160:175], v[208:211], v[128:131], 0\n\t"
      "s_add_u32 m0, s65, 32768\n\t"
      "s_add_u32 s64, s66, s72\n\t"
      "v_accvgpr_write_b32 a40, v8\n\t"
      "v_accvgpr_write_b32 a41, v9\n\t"
      "v_accvgpr_write_b32 a42, v10\n\t"
      "v_accvgpr_write_b32 a43, v11\n\t"
      "v_exp_f32 v56, v56\n\t"
      "v_mfma_f32_32x32x16_bf16 v[160:175], v[212:215], v[128:131], v[160:175]\n\t"
      "buffer_load_dwordx4 v230, s[44:47], s64 offen lds\n\t"
      "v_cvt_pk_bf16_f32 v152, v144, v145\n\t"
      "v_cvt_pk_bf16_f32 v153, v146, v147\n\t"
      "v_cvt_pk_bf16_f32 v154, v148, v149\n\t"
      "v_cvt_pk_bf16_f32 v155, v150, v151\n\t"
      "v_exp_f32 v57, v57\n\t"
      "v_mfma_f32_32x32x16_bf16 v[0:15], v[208:211], v[132:135], 0\n\t"
      "v_accvgpr_write_b32 a44, v152\n\t"
      "v_accvgpr_write_b32 a45, v153\n\t"
      "v_accvgpr_write_b32 a46, v154\n\t"
      "v_accvgpr_write_b32 a47, v155\n\t"
      "v_exp_f32 v58, v58\n\t"
      "v_mfma_f32_32x32x16_bf16 v[0:15], v[212:215], v[132:135], v[0:15]\n\t"
      "v_cvt_pk_bf16_f32 v168, v160, v161\n\t"
      "v_cvt_pk_bf16_f32 v169, v162, v163\n\t"
      "v_cvt_pk_bf16_f32 v170, v164, v165\n\t"
      "v_cvt_pk_bf16_f32 v171, v166, v167\n\t"
      "v_exp_f32 v59, v59\n\t"
      "v_mfma_f32_32x32x16_bf16 v[144:159], v[208:211], v[136:139], 0\n\t"
      "v_accvgpr_write_b32 a48, v168\n\t"
      "v_accvgpr_write_b32 a49, v169\n\t"
      "v_accvgpr_write_b32 a50, v170\n\t"
      "v_accvgpr_write_b32 a51, v171\n\t"
      "v_exp_f32 v60, v60\n\t"
      "v_mfma_f32_32x32x16_bf16 v[144:159], v[212:215], v[136:139], v[144:159]\n\t"
      "buffer_load_dwordx4 v231, s[44:47], s64 offen offset:1024 lds\n\t"
      "v_cvt_pk_bf16_f32 v8, v0, v1\n\t"
      "v_cvt_pk_bf16_f32 v9, v2, v3\n\t"
      "v_cvt_pk_bf16_f32 v10, v4, v5\n\t"
      "v_cvt_pk_bf16_f32 v11, v6, v7\n\t"
      "v_exp_f32 v61, v61\n\t"
      "v_mfma_f32_32x32x16_bf16 v[160:175], v[208:211], v[140:143], 0\n\t"
      "v_accvgpr_write_b32 a52, v8\n\t"
      "v_accvgpr_write_b32 a53, v9\n\t"
      "v_accvgpr_write_b32 a54, v10\n\t"
      "v_accvgpr_write_b32 a55, v11\n\t"
      "v_exp_f32 v62, v62\n\t"
      "v_mfma_f32_32x32x16_bf16 v[160:175], v[212:215], v[140:143], v[160:175]\n\t"
      "v_cvt_pk_bf16_f32 v152, v144, v145\n\t"
      "v_cvt_pk_bf16_f32 v153, v146, v147\n\t"
      "v_cvt_pk_bf16_f32 v154, v148, v149\n\t"
      "v_cvt_pk_bf16_f32 v155, v150, v151\n\t"
      "v_exp_f32 v63, v63\n\t"
      "v_mfma_f32_16x16x32_bf16 v[64:67], a[32:35], v[16:19], 0\n\t"
      "v_accvgpr_write_b32 a56, v152\n\t"
      "v_accvgpr_write_b32 a57, v153\n\t"
      "v_accvgpr_write_b32 a58, v154\n\t"
      "v_accvgpr_write_b32 a59, v155\n\t"
      "v_mfma_f32_16x16x32_bf16 v[64:67], a[36:39], v[20:23], v[64:67]\n\t"
      "s_waitcnt vmcnt(12)\n\t"
      "v_cvt_pk_bf16_f32 v168, v160, v161\n\t"
      "v_cvt_pk_bf16_f32 v169, v162, v163\n\t"
      "v_cvt_pk_bf16_f32 v170, v164, v165\n\t"
      "v_cvt_pk_bf16_f32 v171, v166, v167\n\t"
      "v_add_u32 v236, 0x10000, v236\n\t"
      "v_add_u32 v237, 0x10000, v237\n\t"
      "ds_read_b128 v[112:115], v236 offset:0\n\t"
      "ds_read_b128 v[116:119], v237 offset:0\n\t"
      "ds_read_b128 v[120:123], v236 offset:128\n\t"
      "ds_read_b128 v[124:127], v237 offset:128\n\t"
      "v_mfma_f32_16x16x32_bf16 v[64:67], a[40:43], v[24:27], v[64:67]\n\t"
      "ds_read_b128 v[128:131], v236 offset:4096\n\t"
      "ds_read_b128 v[132:135], v237 offset:4096\n\t"
      "ds_read_b128 v[136:139], v236 offset:4224\n\t"
      "ds_read_b128 v[140:143], v237 offset:4224\n\t"
      "buffer_load_dwordx4 v230, s[44:47], s64 offen offset:2048 lds\n\t"
      "v_accvgpr_write_b32 a60, v168\n\t"
      "v_accvgpr_write_b32 a61, v169\n\t"
      "v_accvgpr_write_b32 a62, v170\n\t"
      "v_accvgpr_write_b32 a63, v171\n\t"
      "v_mfma_f32_16x16x32_bf16 v[64:67], a[44:47], v[28:31], v[64:67]\n\t"
      "v_cvt_pk_bf16_f32 v80, v48, v49\n\t"
      "v_mfma_f32_16x16x32_bf16 v[68:71], a[48:51], v[16:19], 0\n\t"
      "v_cvt_pk_bf16_f32 v81, v50, v51\n\t"
      "v_mfma_f32_16x16x32_bf16 v[68:71], a[52:55], v[20:23], v[68:71]\n\t"
      "v_cvt_pk_bf16_f32 v82, v52, v53\n\t"
      "v_mfma_f32_16x16x32_bf16 v[68:71], a[56:59], v[24:27], v[68:71]\n\t"
      "v_cvt_pk_bf16_f32 v83, v54, v55\n\t"
      "v_mfma_f32_16x16x32_bf16 v[68:71], a[60:63], v[28:31], v[68:71]\n\t"
      "buffer_load_dwordx4 v231, s[44:47], s64 offen offset:3072 lds\n\t"
      "v_cvt_pk_bf16_f32 v96, v56, v57\n\t"
      "v_mfma_f32_16x16x32_bf16 v[72:75], a[32:35], v[32:35], 0\n\t"
      "v_cvt_pk_bf16_f32 v97, v58, v59\n\t"
      "v_mfma_f32_16x16x32_bf16 v[72:75], a[36:39], v[36:39], v[72:75]\n\t"
      "ds_write_b128 v218, v[80:83] offset:0\n\t"
      "v_mfma_f32_16x16x32_bf16 v[72:75], a[40:43], v[40:43], v[72:75]\n\t"
      "s_add_u32 m0, s65, 36864\n\t"
      "s_add_u32 s64, s66, s72\n\t"
      "s_add_u32 s64, s64, 4096\n\t"
      "v_cvt_pk_bf16_f32 v98, v60, v61\n\t"
      "v_mfma_f32_16x16x32_bf16 v[72:75], a[44:47], v[44:47], v[72:75]\n\t"
      "buffer_load_dwordx4 v230, s[44:47], s64 offen lds\n\t"
      "v_cvt_pk_bf16_f32 v99, v62, v63\n\t"
      "v_mfma_f32_16x16x32_bf16 v[76:79], a[48:51], v[32:35], 0\n\t"
      "v_mfma_f32_16x16x32_bf16 v[76:79], a[52:55], v[36:39], v[76:79]\n\t"
      "ds_write_b128 v218, v[96:99] offset:1024\n\t"
      "v_mfma_f32_16x16x32_bf16 v[76:79], a[56:59], v[40:43], v[76:79]\n\t"
      "buffer_load_dwordx4 v231, s[44:47], s64 offen offset:1024 lds\n\t"
      "v_mfma_f32_16x16x32_bf16 v[76:79], a[60:63], v[44:47], v[76:79]\n\t"
      "s_waitcnt lgkmcnt(2)\n\t"
      "v_mfma_f32_32x32x16_bf16 v[144:159], v[208:211], v[112:115], 0\n\t"
      "v_exp_f32 v64, v64\n\t"
      "v_mfma_f32_32x32x16_bf16 v[144:159], v[212:215], v[112:115], v[144:159]\n\t"
      "buffer_load_dwordx4 v230, s[44:47], s64 offen offset:2048 lds\n\t"
      "v_exp_f32 v65, v65\n\t"
      "v_mfma_f32_32x32x16_bf16 v[160:175], v[208:211], v[116:119], 0\n\t"
      "v_exp_f32 v66, v66\n\t"
      "v_mfma_f32_32x32x16_bf16 v[160:175], v[212:215], v[116:119], v[160:175]\n\t"
      "v_cvt_pk_bf16_f32 v152, v144, v145\n\t"
      "v_cvt_pk_bf16_f32 v153, v146, v147\n\t"
      "v_cvt_pk_bf16_f32 v154, v148, v149\n\t"
      "v_cvt_pk_bf16_f32 v155, v150, v151\n\t"
      "v_exp_f32 v67, v67\n\t"
      "v_mfma_f32_32x32x16_bf16 v[0:15], v[208:211], v[120:123], 0\n\t"
      "v_accvgpr_write_b32 a64, v152\n\t"
      "v_accvgpr_write_b32 a65, v153\n\t"
      "v_accvgpr_write_b32 a66, v154\n\t"
      "v_accvgpr_write_b32 a67, v155\n\t"
      "v_exp_f32 v68, v68\n\t"
      "v_mfma_f32_32x32x16_bf16 v[0:15], v[212:215], v[120:123], v[0:15]\n\t"
      "buffer_load_dwordx4 v231, s[44:47], s64 offen offset:3072 lds\n\t"
      "v_cvt_pk_bf16_f32 v168, v160, v161\n\t"
      "v_cvt_pk_bf16_f32 v169, v162, v163\n\t"
      "v_cvt_pk_bf16_f32 v170, v164, v165\n\t"
      "v_cvt_pk_bf16_f32 v171, v166, v167\n\t"
      "v_exp_f32 v69, v69\n\t"
      "v_mfma_f32_32x32x16_bf16 v[144:159], v[208:211], v[124:127], 0\n\t"
      "v_accvgpr_write_b32 a68, v168\n\t"
      "v_accvgpr_write_b32 a69, v169\n\t"
      "v_accvgpr_write_b32 a70, v170\n\t"
      "v_accvgpr_write_b32 a71, v171\n\t"
      "v_exp_f32 v70, v70\n\t"
      "v_mfma_f32_32x32x16_bf16 v[144:159], v[212:215], v[124:127], v[144:159]\n\t"
      "v_cvt_pk_bf16_f32 v8, v0, v1\n\t"
      "v_cvt_pk_bf16_f32 v9, v2, v3\n\t"
      "v_cvt_pk_bf16_f32 v10, v4, v5\n\t"
      "v_cvt_pk_bf16_f32 v11, v6, v7\n\t"
      "v_exp_f32 v71, v71\n\t"
      "v_mfma_f32_32x32x16_bf16 v[160:175], v[208:211], v[128:131], 0\n\t"
      "s_add_u32 m0, s65, 65536\n\t"
      "s_add_u32 s64, s66, s73\n\t"
      "v_accvgpr_write_b32 a72, v8\n\t"
      "v_accvgpr_write_b32 a73, v9\n\t"
      "v_accvgpr_write_b32 a74, v10\n\t"
      "v_accvgpr_write_b32 a75, v11\n\t"
      "v_exp_f32 v72, v72\n\t"
      "v_mfma_f32_32x32x16_bf16 v[160:175], v[212:215], v[128:131], v[160:175]\n\t"
      "buffer_load_dwordx4 v230, s[44:47], s64 offen lds\n\t"
      "v_cvt_pk_bf16_f32 v152, v144, v145\n\t"
      "v_cvt_pk_bf16_f32 v153, v146, v147\n\t"
      "v_cvt_pk_bf16_f32 v154, v148, v149\n\t"
      "v_cvt_pk_bf16_f32 v155, v150, v151\n\t"
      "v_exp_f32 v73, v73\n\t"
      "v_mfma_f32_32x32x16_bf16 v[0:15], v[208:211], v[132:135], 0\n\t"
      "v_accvgpr_write_b32 a76, v152\n\t"
      "v_accvgpr_write_b32 a77, v153\n\t"
      "v_accvgpr_write_b32 a78, v154\n\t"
      "v_accvgpr_write_b32 a79, v155\n\t"
      "v_exp_f32 v74, v74\n\t"
      "v_mfma_f32_32x32x16_bf16 v[0:15], v[212:215], v[132:135], v[0:15]\n\t"
      "v_cvt_pk_bf16_f32 v168, v160, v161\n\t"
      "v_cvt_pk_bf16_f32 v169, v162, v163\n\t"
      "v_cvt_pk_bf16_f32 v170, v164, v165\n\t"
      "v_cvt_pk_bf16_f32 v171, v166, v167\n\t"
      "v_exp_f32 v75, v75\n\t"
      "v_mfma_f32_32x32x16_bf16 v[144:159], v[208:211], v[136:139], 0\n\t"
      "v_accvgpr_write_b32 a80, v168\n\t"
      "v_accvgpr_write_b32 a81, v169\n\t"
      "v_accvgpr_write_b32 a82, v170\n\t"
      "v_accvgpr_write_b32 a83, v171\n\t"
      "v_exp_f32 v76, v76\n\t"
      "v_mfma_f32_32x32x16_bf16 v[144:159], v[212:215], v[136:139], v[144:159]\n\t"
      "buffer_load_dwordx4 v231, s[44:47], s64 offen offset:1024 lds\n\t"
      "v_cvt_pk_bf16_f32 v8, v0, v1\n\t"
      "v_cvt_pk_bf16_f32 v9, v2, v3\n\t"
      "v_cvt_pk_bf16_f32 v10, v4, v5\n\t"
      "v_cvt_pk_bf16_f32 v11, v6, v7\n\t"
      "v_exp_f32 v77, v77\n\t"
      "v_mfma_f32_32x32x16_bf16 v[160:175], v[208:211], v[140:143], 0\n\t"
      "v_accvgpr_write_b32 a84, v8\n\t"
      "v_accvgpr_write_b32 a85, v9\n\t"
      "v_accvgpr_write_b32 a86, v10\n\t"
      "v_accvgpr_write_b32 a87, v11\n\t"
      "v_exp_f32 v78, v78\n\t"
      "v_mfma_f32_32x32x16_bf16 v[160:175], v[212:215], v[140:143], v[160:175]\n\t"
      "v_cvt_pk_bf16_f32 v152, v144, v145\n\t"
      "v_cvt_pk_bf16_f32 v153, v146, v147\n\t"
      "v_cvt_pk_bf16_f32 v154, v148, v149\n\t"
      "v_cvt_pk_bf16_f32 v155, v150, v151\n\t"
      "v_exp_f32 v79, v79\n\t"
      "v_mfma_f32_16x16x32_bf16 v[48:51], a[64:67], v[16:19], 0\n\t"
      "v_accvgpr_write_b32 a88, v152\n\t"
      "v_accvgpr_write_b32 a89, v153\n\t"
      "v_accvgpr_write_b32 a90, v154\n\t"
      "v_accvgpr_write_b32 a91, v155\n\t"
      "v_mfma_f32_16x16x32_bf16 v[48:51], a[68:71], v[20:23], v[48:51]\n\t"
      "s_waitcnt vmcnt(12)\n\t"
      "ds_read_b128 v[112:115], v236 offset:32768\n\t"
      "ds_read_b128 v[116:119], v237 offset:32768\n\t"
      "ds_read_b128 v[120:123], v236 offset:32896\n\t"
      "ds_read_b128 v[124:127], v237 offset:32896\n\t"
      "v_cvt_pk_bf16_f32 v168, v160, v161\n\t"
      "v_cvt_pk_bf16_f32 v169, v162, v163\n\t"
      "v_cvt_pk_bf16_f32 v170, v164, v165\n\t"
      "v_cvt_pk_bf16_f32 v171, v166, v167\n\t"
      "v_mfma_f32_16x16x32_bf16 v[48:51], a[72:75], v[24:27], v[48:51]\n\t"
      "ds_read_b128 v[128:131], v236 offset:36864\n\t"
      "ds_read_b128 v[132:135], v237 offset:36864\n\t"
      "ds_read_b128 v[136:139], v236 offset:36992\n\t"
      "ds_read_b128 v[140:143], v237 offset:36992\n\t"
      "buffer_load_dwordx4 v230, s[44:47], s64 offen offset:2048 lds\n\t"
      "v_accvgpr_write_b32 a92, v168\n\t"
      "v_accvgpr_write_b32 a93, v169\n\t"
      "v_accvgpr_write_b32 a94, v170\n\t"
      "v_accvgpr_write_b32 a95, v171\n\t"
      "v_mfma_f32_16x16x32_bf16 v[48:51], a[76:79], v[28:31], v[48:51]\n\t"
      "v_cvt_pk_bf16_f32 v84, v64, v65\n\t"
      "v_mfma_f32_16x16x32_bf16 v[52:55], a[80:83], v[16:19], 0\n\t"
      "v_cvt_pk_bf16_f32 v85, v66, v67\n\t"
      "v_mfma_f32_16x16x32_bf16 v[52:55], a[84:87], v[20:23], v[52:55]\n\t"
      "v_cvt_pk_bf16_f32 v86, v68, v69\n\t"
      "v_mfma_f32_16x16x32_bf16 v[52:55], a[88:91], v[24:27], v[52:55]\n\t"
      "v_cvt_pk_bf16_f32 v87, v70, v71\n\t"
      "v_mfma_f32_16x16x32_bf16 v[52:55], a[92:95], v[28:31], v[52:55]\n\t"
      "buffer_load_dwordx4 v231, s[44:47], s64 offen offset:3072 lds\n\t"
      "v_cvt_pk_bf16_f32 v100, v72, v73\n\t"
      "v_mfma_f32_16x16x32_bf16 v[56:59], a[64:67], v[32:35], 0\n\t"
      "v_cvt_pk_bf16_f32 v101, v74, v75\n\t"
      "v_mfma_f32_16x16x32_bf16 v[56:59], a[68:71], v[36:39], v[56:59]\n\t"
      "ds_write_b128 v218, v[84:87] offset:2048\n\t"
      "v_mfma_f32_16x16x32_bf16 v[56:59], a[72:75], v[40:43], v[56:59]\n\t"
      "s_add_u32 m0, s65, 69632\n\t"
      "s_add_u32 s64, s66, s73\n\t"
      "s_add_u32 s64, s64, 4096\n\t"
      "v_cvt_pk_bf16_f32 v102, v76, v77\n\t"
      "v_mfma_f32_16x16x32_bf16 v[56:59], a[76:79], v[44:47], v[56:59]\n\t"
      "buffer_load_dwordx4 v230, s[44:47], s64 offen lds\n\t"
      "v_cvt_pk_bf16_f32 v103, v78, v79\n\t"
      "v_mfma_f32_16x16x32_bf16 v[60:63], a[80:83], v[32:35], 0\n\t"
      "v_mfma_f32_16x16x32_bf16 v[60:63], a[84:87], v[36:39], v[60:63]\n\t"
      "ds_write_b128 v218, v[100:103] offset:3072\n\t"
      "v_smfmac_f32_16x16x64_bf16 v[184:187], v[192:195], v[80:87], v196\n\t"
      "v_mfma_f32_16x16x32_bf16 v[60:63], a[88:91], v[40:43], v[60:63]\n\t"
      "buffer_load_dwordx4 v231, s[44:47], s64 offen offset:1024 lds\n\t"
      "v_smfmac_f32_16x16x64_bf16 v[188:191], v[192:195], v[96:103], v196\n\t"
      "v_mfma_f32_16x16x32_bf16 v[60:63], a[92:95], v[44:47], v[60:63]\n\t"
      "s_waitcnt lgkmcnt(2)\n\t"
      "v_mfma_f32_32x32x16_bf16 v[144:159], v[208:211], v[112:115], 0\n\t"
      "v_exp_f32 v48, v48\n\t"
      "v_mfma_f32_32x32x16_bf16 v[144:159], v[212:215], v[112:115], v[144:159]\n\t"
      "buffer_load_dwordx4 v230, s[44:47], s64 offen offset:2048 lds\n\t"
      "v_exp_f32 v49, v49\n\t"
      "v_mfma_f32_32x32x16_bf16 v[160:175], v[208:211], v[116:119], 0\n\t"
      "v_exp_f32 v50, v50\n\t"
      "v_mfma_f32_32x32x16_bf16 v[160:175], v[212:215], v[116:119], v[160:175]\n\t"
      "v_cvt_pk_bf16_f32 v152, v144, v145\n\t"
      "v_cvt_pk_bf16_f32 v153, v146, v147\n\t"
      "v_cvt_pk_bf16_f32 v154, v148, v149\n\t"
      "v_cvt_pk_bf16_f32 v155, v150, v151\n\t"
      "v_exp_f32 v51, v51\n\t"
      "v_mfma_f32_32x32x16_bf16 v[0:15], v[208:211], v[120:123], 0\n\t"
      "v_accvgpr_write_b32 a96, v152\n\t"
      "v_accvgpr_write_b32 a97, v153\n\t"
      "v_accvgpr_write_b32 a98, v154\n\t"
      "v_accvgpr_write_b32 a99, v155\n\t"
      "v_exp_f32 v52, v52\n\t"
      "v_mfma_f32_32x32x16_bf16 v[0:15], v[212:215], v[120:123], v[0:15]\n\t"
      "buffer_load_dwordx4 v231, s[44:47], s64 offen offset:3072 lds\n\t"
      "v_cvt_pk_bf16_f32 v168, v160, v161\n\t"
      "v_cvt_pk_bf16_f32 v169, v162, v163\n\t"
      "v_cvt_pk_bf16_f32 v170, v164, v165\n\t"
      "v_cvt_pk_bf16_f32 v171, v166, v167\n\t"
      "v_exp_f32 v53, v53\n\t"
      "v_mfma_f32_32x32x16_bf16 v[144:159], v[208:211], v[124:127], 0\n\t"
      "v_accvgpr_write_b32 a100, v168\n\t"
      "v_accvgpr_write_b32 a101, v169\n\t"
      "v_accvgpr_write_b32 a102, v170\n\t"
      "v_accvgpr_write_b32 a103, v171\n\t"
      "v_exp_f32 v54, v54\n\t"
      "v_mfma_f32_32x32x16_bf16 v[144:159], v[212:215], v[124:127], v[144:159]\n\t"
      "v_cvt_pk_bf16_f32 v8, v0, v1\n\t"
      "v_cvt_pk_bf16_f32 v9, v2, v3\n\t"
      "v_cvt_pk_bf16_f32 v10, v4, v5\n\t"
      "v_cvt_pk_bf16_f32 v11, v6, v7\n\t"
      "v_exp_f32 v55, v55\n\t"
      "v_mfma_f32_32x32x16_bf16 v[160:175], v[208:211], v[128:131], 0\n\t"
      "s_add_u32 m0, s55, s56\n\t"
      "v_accvgpr_write_b32 a104, v8\n\t"
      "v_accvgpr_write_b32 a105, v9\n\t"
      "v_accvgpr_write_b32 a106, v10\n\t"
      "v_accvgpr_write_b32 a107, v11\n\t"
      "v_exp_f32 v56, v56\n\t"
      "v_mfma_f32_32x32x16_bf16 v[160:175], v[212:215], v[128:131], v[160:175]\n\t"
      "buffer_load_dwordx4 v226, s[36:39], s54 offen nt lds\n\t"
      "v_cvt_pk_bf16_f32 v152, v144, v145\n\t"
      "v_cvt_pk_bf16_f32 v153, v146, v147\n\t"
      "v_cvt_pk_bf16_f32 v154, v148, v149\n\t"
      "v_cvt_pk_bf16_f32 v155, v150, v151\n\t"
      "v_exp_f32 v57, v57\n\t"
      "v_mfma_f32_32x32x16_bf16 v[0:15], v[208:211], v[132:135], 0\n\t"
      "v_accvgpr_write_b32 a108, v152\n\t"
      "v_accvgpr_write_b32 a109, v153\n\t"
      "v_accvgpr_write_b32 a110, v154\n\t"
      "v_accvgpr_write_b32 a111, v155\n\t"
      "v_exp_f32 v58, v58\n\t"
      "v_mfma_f32_32x32x16_bf16 v[0:15], v[212:215], v[132:135], v[0:15]\n\t"
      "v_cvt_pk_bf16_f32 v168, v160, v161\n\t"
      "v_cvt_pk_bf16_f32 v169, v162, v163\n\t"
      "v_cvt_pk_bf16_f32 v170, v164, v165\n\t"
      "v_cvt_pk_bf16_f32 v171, v166, v167\n\t"
      "v_exp_f32 v59, v59\n\t"
      "v_mfma_f32_32x32x16_bf16 v[144:159], v[208:211], v[136:139], 0\n\t"
      "s_add_u32 m0, s55, s57\n\t"
      "v_accvgpr_write_b32 a112, v168\n\t"
      "v_accvgpr_write_b32 a113, v169\n\t"
      "v_accvgpr_write_b32 a114, v170\n\t"
      "v_accvgpr_write_b32 a115, v171\n\t"
      "v_exp_f32 v60, v60\n\t"
      "v_mfma_f32_32x32x16_bf16 v[144:159], v[212:215], v[136:139], v[144:159]\n\t"
      "buffer_load_dwordx4 v226, s[36:39], s54 offen offset:1024 nt lds\n\t"
      "s_add_u32 s55, s55, 8704\n\t"
      "s_cmp_eq_u32 s55, 26112\n\t"
      "s_cselect_b32 s55, 0, s55\n\t"
      "s_add_u32 s54, s54, 8192\n\t"
      "v_cvt_pk_bf16_f32 v8, v0, v1\n\t"
      "v_cvt_pk_bf16_f32 v9, v2, v3\n\t"
      "v_cvt_pk_bf16_f32 v10, v4, v5\n\t"
      "v_cvt_pk_bf16_f32 v11, v6, v7\n\t"
      "v_exp_f32 v61, v61\n\t"
      "v_mfma_f32_32x32x16_bf16 v[160:175], v[208:211], v[140:143], 0\n\t"
      "v_accvgpr_write_b32 a116, v8\n\t"
      "v_accvgpr_write_b32 a117, v9\n\t"
      "v_accvgpr_write_b32 a118, v10\n\t"
      "v_accvgpr_write_b32 a119, v11\n\t"
      "v_exp_f32 v62, v62\n\t"
      "v_mfma_f32_32x32x16_bf16 v[160:175], v[212:215], v[140:143], v[160:175]\n\t"
      "v_cvt_pk_bf16_f32 v152, v144, v145\n\t"
      "v_cvt_pk_bf16_f32 v153, v146, v147\n\t"
      "v_cvt_pk_bf16_f32 v154, v148, v149\n\t"
      "v_cvt_pk_bf16_f32 v155, v150, v151\n\t"
      "v_exp_f32 v63, v63\n\t"
      "v_mfma_f32_16x16x32_bf16 v[64:67], a[96:99], v[16:19], 0\n\t"
      "v_accvgpr_write_b32 a120, v152\n\t"
      "v_accvgpr_write_b32 a121, v153\n\t"
      "v_accvgpr_write_b32 a122, v154\n\t"
      "v_accvgpr_write_b32 a123, v155\n\t"
      "v_mfma_f32_16x16x32_bf16 v[64:67], a[100:103], v[20:23], v[64:67]\n\t"
      "s_add_u32 m0, s65, 98304\n\t"
      "s_add_u32 s64, s66, s74\n\t"
      "v_cvt_pk_bf16_f32 v168, v160, v161\n\t"
      "v_cvt_pk_bf16_f32 v169, v162, v163\n\t"
      "v_cvt_pk_bf16_f32 v170, v164, v165\n\t"
      "v_cvt_pk_bf16_f32 v171, v166, v167\n\t"
      "v_mfma_f32_16x16x32_bf16 v[64:67], a[104:107], v[24:27], v[64:67]\n\t"
      "buffer_load_dwordx4 v230, s[44:47], s64 offen lds\n\t"
      "v_accvgpr_write_b32 a124, v168\n\t"
      "v_accvgpr_write_b32 a125, v169\n\t"
      "v_accvgpr_write_b32 a126, v170\n\t"
      "v_accvgpr_write_b32 a127, v171\n\t"
      "v_mfma_f32_16x16x32_bf16 v[64:67], a[108:111], v[28:31], v[64:67]\n\t"
      "v_cvt_pk_bf16_f32 v88, v48, v49\n\t"
      "v_mfma_f32_16x16x32_bf16 v[68:71], a[112:115], v[16:19], 0\n\t"
      "v_cvt_pk_bf16_f32 v89, v50, v51\n\t"
      "v_mfma_f32_16x16x32_bf16 v[68:71], a[116:119], v[20:23], v[68:71]\n\t"
      "v_cvt_pk_bf16_f32 v90, v52, v53\n\t"
      "v_mfma_f32_16x16x32_bf16 v[68:71], a[120:123], v[24:27], v[68:71]\n\t"
      "v_cvt_pk_bf16_f32 v91, v54, v55\n\t"
      "v_mfma_f32_16x16x32_bf16 v[68:71], a[124:127], v[28:31], v[68:71]\n\t"
      "buffer_load_dwordx4 v231, s[44:47], s64 offen offset:1024 lds\n\t"
      "v_cvt_pk_bf16_f32 v104, v56, v57\n\t"
      "v_mfma_f32_16x16x32_bf16 v[72:75], a[96:99], v[32:35], 0\n\t"
      "v_cvt_pk_bf16_f32 v105, v58, v59\n\t"
      "v_mfma_f32_16x16x32_bf16 v[72:75], a[100:103], v[36:39], v[72:75]\n\t"
      "ds_write_b128 v218, v[88:91] offset:4096\n\t"
      "v_mfma_f32_16x16x32_bf16 v[72:75], a[104:107], v[40:43], v[72:75]\n\t"
      "v_cvt_pk_bf16_f32 v106, v60, v61\n\t"
      "v_mfma_f32_16x16x32_bf16 v[72:75], a[108:111], v[44:47], v[72:75]\n\t"
      "buffer_load_dwordx4 v230, s[44:47], s64 offen offset:2048 lds\n\t"
      "v_cvt_pk_bf16_f32 v107, v62, v63\n\t"
      "v_mfma_f32_16x16x32_bf16 v[76:79], a[112:115], v[32:35], 0\n\t"
      "v_mfma_f32_16x16x32_bf16 v[76:79], a[116:119], v[36:39], v[76:79]\n\t"
      "ds_write_b128 v218, v[104:107] offset:5120\n\t"
      "s_waitcnt vmcnt(21)\n\t"
      "s_barrier\n\t"
      "v_mfma_f32_16x16x32_bf16 v[76:79], a[120:123], v[40:43], v[76:79]\n\t"
      "buffer_load_dwordx4 v231, s[44:47], s64 offen offset:3072 lds\n\t"
      "v_mfma_f32_16x16x32_bf16 v[76:79], a[124:127], v[44:47], v[76:79]\n\t"
      "v_add_u32 v216, 8704, v217\n\t"
      "s_add_u32 m0, s55, s56\n\t"
      "ds_read_b128 v[16:19], v216 offset:0\n\t"
      "ds_read_b128 v[20:23], v216 offset:64\n\t"
      "ds_read_b128 v[24:27], v216 offset:128\n\t"
      "ds_read_b128 v[28:31], v216 offset:192\n\t"
      "buffer_load_dwordx4 v226, s[36:39], s54 offen nt lds\n\t"
      "s_add_u32 m0, s55, s57\n\t"
      "ds_read_b128 v[32:35], v216 offset:4352\n\t"
      "ds_read_b128 v[36:39], v216 offset:4416\n\t"
      "ds_read_b128 v[40:43], v216 offset:4480\n\t"
      "ds_read_b128 v[44:47], v216 offset:4544\n\t"
      "buffer_load_dwordx4 v226, s[36:39], s54 offen offset:1024 nt lds\n\t"
      "s_add_u32 s55, s55, 8704\n\t"
      "s_cmp_eq_u32 s55, 26112\n\t"
      "s_cselect_b32 s55, 0, s55\n\t"
      "s_add_u32 s54, s54, 8192\n\t"
      "v_mov_b32 v228, 1\n\t"
      "s_waitcnt lgkmcnt(0)\n\t"
      "v_mfma_f32_16x16x32_bf16 v[48:51], a[0:3], v[16:19], 0\n\t"
      "v_exp_f32 v64, v64\n\t"
      "s_add_u32 m0, s65, 102400\n\t"
      "s_add_u32 s64, s66, s74\n\t"
      "s_add_u32 s64, s64, 4096\n\t"
      "v_mfma_f32_16x16x32_bf16 v[48:51], a[4:7], v[20:23], v[48:51]\n\t"
      "buffer_load_dwordx4 v230, s[44:47], s64 offen lds\n\t"
      "v_exp_f32 v65, v65\n\t"
      "v_mfma_f32_16x16x32_bf16 v[48:51], a[8:11], v[24:27], v[48:51]\n\t"
      "v_exp_f32 v66, v66\n\t"
      "v_mfma_f32_16x16x32_bf16 v[48:51], a[12:15], v[28:31], v[48:51]\n\t"
      "v_exp_f32 v67, v67\n\t"
      "v_mfma_f32_16x16x32_bf16 v[52:55], a[16:19], v[16:19], 0\n\t"
      "v_exp_f32 v68, v68\n\t"
      "v_cvt_pk_bf16_f32 v92, v64, v65\n\t"
      "v_mfma_f32_16x16x32_bf16 v[52:55], a[20:23], v[20:23], v[52:55]\n\t"
      "buffer_load_dwordx4 v231, s[44:47], s64 offen offset:1024 lds\n\t"
      "v_exp_f32 v69, v69\n\t"
      "v_cvt_pk_bf16_f32 v93, v66, v67\n\t"
      "v_mfma_f32_16x16x32_bf16 v[52:55], a[24:27], v[24:27], v[52:55]\n\t"
      "v_exp_f32 v70, v70\n\t"
      "v_mfma_f32_16x16x32_bf16 v[52:55], a[28:31], v[28:31], v[52:55]\n\t"
      "v_exp_f32 v71, v71\n\t"
      "v_mfma_f32_16x16x32_bf16 v[56:59], a[0:3], v[32:35], 0\n\t"
      "v_exp_f32 v72, v72\n\t"
      "v_cvt_pk_bf16_f32 v94, v68, v69\n\t"
      "v_exp_f32 v48, v48\n\t"
      "v_mfma_f32_16x16x32_bf16 v[56:59], a[4:7], v[36:39], v[56:59]\n\t"
      "v_exp_f32 v73, v73\n\t"
      "v_cvt_pk_bf16_f32 v95, v70, v71\n\t"
      "v_mfma_f32_16x16x32_bf16 v[56:59], a[8:11], v[40:43], v[56:59]\n\t"
      "v_exp_f32 v74, v74\n\t"
      "v_exp_f32 v49, v49\n\t"
      "v_mfma_f32_16x16x32_bf16 v[56:59], a[12:15], v[44:47], v[56:59]\n\t"
      "ds_write_b128 v218, v[92:95] offset:6144\n\t"
      "buffer_load_dwordx4 v230, s[44:47], s64 offen offset:2048 lds\n\t"
      "v_exp_f32 v75, v75\n\t"
      "v_mfma_f32_16x16x32_bf16 v[60:63], a[16:19], v[32:35], 0\n\t"
      "v_exp_f32 v76, v76\n\t"
      "v_cvt_pk_bf16_f32 v108, v72, v73\n\t"
      "v_exp_f32 v50, v50\n\t"
      "v_mfma_f32_16x16x32_bf16 v[60:63], a[20:23], v[36:39], v[60:63]\n\t"
      "v_exp_f32 v77, v77\n\t"
      "v_cvt_pk_bf16_f32 v109, v74, v75\n\t"
      "v_smfmac_f32_16x16x64_bf16 v[184:187], v[192:195], v[88:95], v196\n\t"
      "v_mfma_f32_16x16x32_bf16 v[60:63], a[24:27], v[40:43], v[60:63]\n\t"
      "v_exp_f32 v78, v78\n\t"
      "v_exp_f32 v51, v51\n\t"
      "v_mfma_f32_16x16x32_bf16 v[60:63], a[28:31], v[44:47], v[60:63]\n\t"
      "v_exp_f32 v79, v79\n\t"
      "v_mfma_f32_16x16x32_bf16 v[64:67], a[32:35], v[16:19], 0\n\t"
      "v_cvt_pk_bf16_f32 v110, v76, v77\n\t"
      "v_exp_f32 v52, v52\n\t"
      "v_mfma_f32_16x16x32_bf16 v[64:67], a[36:39], v[20:23], v[64:67]\n\t"
      "buffer_load_dwordx4 v231, s[44:47], s64 offen offset:3072 lds\n\t"
      "v_cvt_pk_bf16_f32 v111, v78, v79\n\t"
      "v_add_f32 v201, v184, v185\n\t"
      "v_mfma_f32_16x16x32_bf16 v[64:67], a[40:43], v[24:27], v[64:67]\n\t"
      "ds_write_b128 v218, v[108:111] offset:7168\n\t"
      "v_mov_b32 v184, 0\n\t"
      "v_mov_b32 v185, 0\n\t"
      "v_exp_f32 v53, v53\n\t"
      "v_mfma_f32_16x16x32_bf16 v[64:67], a[44:47], v[28:31], v[64:67]\n\t"
      "s_waitcnt vmcnt(12) lgkmcnt(0)\n\t"
      "s_barrier\n\t"
      "v_smfmac_f32_16x16x64_bf16 v[188:191], v[192:195], v[104:111], v196\n\t"
      "v_mfma_f32_16x16x32_bf16 v[68:71], a[48:51], v[16:19], 0\n\t"
      "ds_read_b64_tr_b16 a[128:129], v246 offset:0\n\t"
      "ds_read_b64_tr_b16 a[130:131], v246 offset:4096\n\t"
      "ds_read_b64_tr_b16 a[132:133], v247 offset:0\n\t"
      "ds_read_b64_tr_b16 a[134:135], v247 offset:4096\n\t"
      "v_exp_f32 v54, v54\n\t"
      "v_mfma_f32_16x16x32_bf16 v[68:71], a[52:55], v[20:23], v[68:71]\n\t"
      "ds_read_b64_tr_b16 a[160:161], v248 offset:0\n\t"
      "ds_read_b64_tr_b16 a[162:163], v248 offset:4096\n\t"
      "ds_read_b64_tr_b16 a[164:165], v249 offset:0\n\t"
      "ds_read_b64_tr_b16 a[166:167], v249 offset:4096\n\t"
      "ds_read_b128 v[112:115], v219 offset:0\n\t"
      "v_mfma_f32_16x16x32_bf16 v[68:71], a[56:59], v[24:27], v[68:71]\n\t"
      "ds_read_b64_tr_b16 a[192:193], v250 offset:0\n\t"
      "ds_read_b64_tr_b16 a[194:195], v250 offset:4096\n\t"
      "ds_read_b64_tr_b16 a[196:197], v251 offset:0\n\t"
      "ds_read_b64_tr_b16 a[198:199], v251 offset:4096\n\t"
      "v_exp_f32 v55, v55\n\t"
      "v_mfma_f32_16x16x32_bf16 v[68:71], a[60:63], v[28:31], v[68:71]\n\t"
      "ds_read_b64_tr_b16 a[224:225], v252 offset:0\n\t"
      "ds_read_b64_tr_b16 a[226:227], v252 offset:4096\n\t"
      "ds_read_b64_tr_b16 a[228:229], v253 offset:0\n\t"
      "ds_read_b64_tr_b16 a[230:231], v253 offset:4096\n\t"
      "ds_read_b128 v[116:119], v219 offset:1024\n\t"
      "v_mfma_f32_16x16x32_bf16 v[72:75], a[32:35], v[32:35], 0\n\t"
      "ds_read_b64_tr_b16 a[136:137], v246 offset:32768\n\t"
      "ds_read_b64_tr_b16 a[138:139], v246 offset:36864\n\t"
      "ds_read_b64_tr_b16 a[140:141], v247 offset:32768\n\t"
      "ds_read_b64_tr_b16 a[142:143], v247 offset:36864\n\t"
      "ds_read_b128 v[120:123], v220 offset:0\n\t"
      "v_exp_f32 v56, v56\n\t"
      "v_mfma_f32_16x16x32_bf16 v[72:75], a[36:39], v[36:39], v[72:75]\n\t"
      "ds_read_b64_tr_b16 a[168:169], v248 offset:32768\n\t"
      "ds_read_b64_tr_b16 a[170:171], v248 offset:36864\n\t"
      "ds_read_b64_tr_b16 a[172:173], v249 offset:32768\n\t"
      "ds_read_b64_tr_b16 a[174:175], v249 offset:36864\n\t"
      "ds_read_b128 v[124:127], v220 offset:1024\n\t"
      "v_mfma_f32_16x16x32_bf16 v[72:75], a[40:43], v[40:43], v[72:75]\n\t"
      "ds_read_b64_tr_b16 a[200:201], v250 offset:32768\n\t"
      "ds_read_b64_tr_b16 a[202:203], v250 offset:36864\n\t"
      "ds_read_b64_tr_b16 a[204:205], v251 offset:32768\n\t"
      "ds_read_b64_tr_b16 a[206:207], v251 offset:36864\n\t"
      "ds_read_b128 v[128:131], v221 offset:0\n\t"
      "v_exp_f32 v57, v57\n\t"
      "v_mfma_f32_16x16x32_bf16 v[72:75], a[44:47], v[44:47], v[72:75]\n\t"
      "ds_read_b64_tr_b16 a[232:233], v252 offset:32768\n\t"
      "ds_read_b64_tr_b16 a[234:235], v252 offset:36864\n\t"
      "ds_read_b64_tr_b16 a[236:237], v253 offset:32768\n\t"
      "ds_read_b64_tr_b16 a[238:239], v253 offset:36864\n\t"
      "ds_read_b128 v[132:135], v221 offset:1024\n\t"
      "v_mfma_f32_16x16x32_bf16 v[76:79], a[48:51], v[32:35], 0\n\t"
      "v_exp_f32 v58, v58\n\t"
      "v_mfma_f32_16x16x32_bf16 v[76:79], a[52:55], v[36:39], v[76:79]\n\t"
      "s_waitcnt lgkmcnt(0)\n\t"
      "s_barrier\n\t"
      "v_mfma_f32_16x16x32_bf16 v[76:79], a[56:59], v[40:43], v[76:79]\n\t"
      "v_exp_f32 v59, v59\n\t"
      "v_add_u32 v246, 0x8000, v246\n\t"
      "v_add_u32 v247, 0x8000, v247\n\t"
      "v_add_u32 v248, 0x8000, v248\n\t"
      "v_add_u32 v249, 0x8000, v249\n\t"
      "s_add_u32 m0, s65, 65536\n\t"
      "s_add_u32 s64, s66, s75\n\t"
      "v_mfma_f32_16x16x32_bf16 v[76:79], a[60:63], v[44:47], v[76:79]\n\t"
      "buffer_load_dwordx4 v230, s[44:47], s64 offen lds\n\t"
      "v_add_f32 v202, v188, v189\n\t"
      "v_cndmask_b32 v201, v201, v202, s[60:61]\n\t"
      "v_mfma_f32_16x16x32_bf16 v[144:147], a[128:131], v[80:83], 0\n\t"
      "v_exp_f32 v60, v60\n\t"
      "v_add_u32 v250, 0x8000, v250\n\t"
      "v_add_u32 v251, 0x8000, v251\n\t"
      "v_add_u32 v252, 0x8000, v252\n\t"
      "v_add_u32 v253, 0x8000, v253\n\t"
      "v_mfma_f32_16x16x32_bf16 v[148:151], a[132:135], v[80:83], 0\n\t"
      "ds_write_b32 v224, v201 offset:0\n\t"
      "ds_read_b128 v[160:163], v219 offset:2048\n\t"
      "ds_read_b128 v[164:167], v219 offset:3072\n\t"
      "v_mfma_f32_16x16x32_bf16 v[152:155], a[128:131], v[96:99], 0\n\t"
      "buffer_load_dwordx4 v231, s[44:47], s64 offen offset:1024 lds\n\t"
      "v_exp_f32 v61, v61\n\t"
      "v_mfma_f32_16x16x32_bf16 v[156:159], a[132:135], v[96:99], 0\n\t"
      "ds_read_b128 v[168:171], v220 offset:2048\n\t"
      "ds_read_b128 v[172:175], v220 offset:3072\n\t"
      "v_mov_b32 v188, 0\n\t"
      "v_mov_b32 v189, 0\n\t"
      "v_cvt_pk_bf16_f32 v80, v48, v49\n\t"
      "v_cvt_pk_bf16_f32 v81, v50, v51\n\t"
      "v_mfma_f32_16x16x32_bf16 v[144:147], a[160:163], v[112:115], v[144:147]\n\t"
      "v_exp_f32 v62, v62\n\t"
      "v_mfma_f32_16x16x32_bf16 v[148:151], a[164:167], v[112:115], v[148:151]\n\t"
      "ds_read_b128 v[176:179], v221 offset:2048\n\t"
      "ds_read_b128 v[180:183], v221 offset:3072\n\t"
      "buffer_load_dwordx4 v230, s[44:47], s64 offen offset:2048 lds\n\t"
      "v_cvt_pk_bf16_f32 v82, v52, v53\n\t"
      "v_cvt_pk_bf16_f32 v83, v54, v55\n\t"
      "v_mfma_f32_16x16x32_bf16 v[152:155], a[160:163], v[116:119], v[152:155]\n\t"
      "v_exp_f32 v63, v63\n\t"
      "v_mfma_f32_16x16x32_bf16 v[156:159], a[164:167], v[116:119], v[156:159]\n\t"
      "v_cvt_pk_bf16_f32 v96, v56, v57\n\t"
      "v_cvt_pk_bf16_f32 v97, v58, v59\n\t"
      "v_mfma_f32_16x16x32_bf16 v[144:147], a[192:195], v[120:123], v[144:147]\n\t"
      "buffer_load_dwordx4 v231, s[44:47], s64 offen offset:3072 lds\n\t"
      "v_exp_f32 v64, v64\n\t"
      "v_mfma_f32_16x16x32_bf16 v[148:151], a[196:199], v[120:123], v[148:151]\n\t"
      "v_cvt_pk_bf16_f32 v98, v60, v61\n\t"
      "v_mfma_f32_16x16x32_bf16 v[152:155], a[192:195], v[124:127], v[152:155]\n\t"
      "v_exp_f32 v65, v65\n\t"
      "v_mfma_f32_16x16x32_bf16 v[156:159], a[196:199], v[124:127], v[156:159]\n\t"
      "v_cvt_pk_bf16_f32 v99, v62, v63\n\t"
      "s_waitcnt lgkmcnt(0)\n\t"
      "s_barrier\n\t"
      "s_add_u32 m0, s65, 69632\n\t"
      "s_add_u32 s64, s66, s75\n\t"
      "s_add_u32 s64, s64, 4096\n\t"
      "v_mfma_f32_16x16x32_bf16 v[144:147], a[224:227], v[128:131], v[144:147]\n\t"
      "ds_read_b128 v[204:207], v225 offset:0\n\t"
      "buffer_load_dwordx4 v230, s[44:47], s64 offen lds\n\t"
      "v_exp_f32 v66, v66\n\t"
      "v_mfma_f32_16x16x32_bf16 v[148:151], a[228:231], v[128:131], v[148:151]\n\t"
      "v_mfma_f32_16x16x32_bf16 v[152:155], a[224:227], v[132:135], v[152:155]\n\t"
      "v_exp_f32 v67, v67\n\t"
      "v_mfma_f32_16x16x32_bf16 v[156:159], a[228:231], v[132:135], v[156:159]\n\t"
      "ds_write_b128 v218, v[80:83] offset:32768\n\t"
      "v_mfma_f32_16x16x32_bf16 v[144:147], a[136:139], v[84:87], v[144:147]\n\t"
      "buffer_load_dwordx4 v231, s[44:47], s64 offen offset:1024 lds\n\t"
      "v_exp_f32 v68, v68\n\t"
      "v_mfma_f32_16x16x32_bf16 v[148:151], a[140:143], v[84:87], v[148:151]\n\t"
      "s_waitcnt lgkmcnt(1)\n\t"
      "v_add_f32 v204, v204, v205\n\t"
      "v_add_f32 v206, v206, v207\n\t"
      "v_mfma_f32_16x16x32_bf16 v[152:155], a[136:139], v[100:103], v[152:155]\n\t"
      "v_exp_f32 v69, v69\n\t"
      "v_mfma_f32_16x16x32_bf16 v[156:159], a[140:143], v[100:103], v[156:159]\n\t"
      "v_cvt_pk_bf16_f32 v84, v64, v65\n\t"
      "v_cvt_pk_bf16_f32 v85, v66, v67\n\t"
      "v_mfma_f32_16x16x32_bf16 v[144:147], a[168:171], v[160:163], v[144:147]\n\t"
      "ds_write_b128 v218, v[96:99] offset:33792\n\t"
      "buffer_load_dwordx4 v230, s[44:47], s64 offen offset:2048 lds\n\t"
      "v_exp_f32 v70, v70\n\t"
      "v_mfma_f32_16x16x32_bf16 v[148:151], a[172:175], v[160:163], v[148:151]\n\t"
      "v_cvt_pk_bf16_f32 v86, v68, v69\n\t"
      "v_add_f32 v204, v204, v206\n\t"
      "v_mfma_f32_16x16x32_bf16 v[152:155], a[168:171], v[164:167], v[152:155]\n\t"
      "v_exp_f32 v71, v71\n\t"
      "v_mfma_f32_16x16x32_bf16 v[156:159], a[172:175], v[164:167], v[156:159]\n\t"
      "v_cvt_pk_bf16_f32 v87, v70, v71\n\t"
      "v_rcp_f32 v197, v204\n\t"
      "v_mfma_f32_16x16x32_bf16 v[144:147], a[200:203], v[168:171], v[144:147]\n\t"
      "buffer_load_dwordx4 v231, s[44:47], s64 offen offset:3072 lds\n\t"
      "v_exp_f32 v72, v72\n\t"
      "v_mfma_f32_16x16x32_bf16 v[148:151], a[204:207], v[168:171], v[148:151]\n\t"
      "ds_write_b128 v218, v[84:87] offset:34816\n\t"
      "v_mfma_f32_16x16x32_bf16 v[152:155], a[200:203], v[172:175], v[152:155]\n\t"
      "v_exp_f32 v73, v73\n\t"
      "v_smfmac_f32_16x16x64_bf16 v[184:187], v[192:195], v[80:87], v196\n\t"
      "v_mfma_f32_16x16x32_bf16 v[156:159], a[204:207], v[172:175], v[156:159]\n\t"
      "v_cvt_pk_bf16_f32 v100, v72, v73\n\t"
      "v_mfma_f32_16x16x32_bf16 v[144:147], a[232:235], v[176:179], v[144:147]\n\t"
      "v_exp_f32 v74, v74\n\t"
      "v_mfma_f32_16x16x32_bf16 v[148:151], a[236:239], v[176:179], v[148:151]\n\t"
      "v_mov_b32 v198, v197\n\t"
      "v_max3_u32 v200, v200, v204, v197\n\t"
      "v_mfma_f32_16x16x32_bf16 v[152:155], a[232:235], v[180:183], v[152:155]\n\t"
      "ds_read_b128 v[112:115], v219 offset:4096\n\t"
      "ds_read_b128 v[116:119], v219 offset:5120\n\t"
      "v_exp_f32 v75, v75\n\t"
      "v_mfma_f32_16x16x32_bf16 v[156:159], a[236:239], v[180:183], v[156:159]\n\t"
      "v_cvt_pk_bf16_f32 v101, v74, v75\n\t"
      "v_permlane16_swap_b32 v197, v198\n\t"
      "v_mfma_f32_16x16x32_bf16 v[48:51], a[64:67], v[16:19], 0\n\t"
      "ds_read_b128 v[120:123], v220 offset:4096\n\t"
      "ds_read_b128 v[124:127], v220 offset:5120\n\t"
      "v_exp_f32 v76, v76\n\t"
      "v_mfma_f32_16x16x32_bf16 v[48:51], a[68:71], v[20:23], v[48:51]\n\t"
      "v_mfma_f32_16x16x32_bf16 v[48:51], a[72:75], v[24:27], v[48:51]\n\t"
      "ds_read_b128 v[128:131], v221 offset:4096\n\t"
      "ds_read_b128 v[132:135], v221 offset:5120\n\t"
      "v_exp_f32 v77, v77\n\t"
      "v_mfma_f32_16x16x32_bf16 v[48:51], a[76:79], v[28:31], v[48:51]\n\t"
      "v_cvt_pk_bf16_f32 v102, v76, v77\n\t"
      "v_mfma_f32_16x16x32_bf16 v[52:55], a[80:83], v[16:19], 0\n\t"
      "v_exp_f32 v78, v78\n\t"
      "v_mfma_f32_16x16x32_bf16 v[52:55], a[84:87], v[20:23], v[52:55]\n\t"
      "v_mfma_f32_16x16x32_bf16 v[52:55], a[88:91], v[24:27], v[52:55]\n\t"
      "s_waitcnt vmcnt(8)\n\t"
      "s_barrier\n\t"
      "ds_read_b64_tr_b16 a[144:145], v246 offset:32768\n\t"
      "ds_read_b64_tr_b16 a[146:147], v246 offset:36864\n\t"
      "v_exp_f32 v79, v79\n\t"
      "v_mfma_f32_16x16x32_bf16 v[52:55], a[92:95], v[28:31], v[52:55]\n\t"
      "ds_read_b64_tr_b16 a[148:149], v247 offset:32768\n\t"
      "ds_read_b64_tr_b16 a[150:151], v247 offset:36864\n\t"
      "v_cvt_pk_bf16_f32 v103, v78, v79\n\t"
      "v_mfma_f32_16x16x32_bf16 v[56:59], a[64:67], v[32:35], 0\n\t"
      "ds_read_b64_tr_b16 a[176:177], v248 offset:32768\n\t"
      "ds_read_b64_tr_b16 a[178:179], v248 offset:36864\n\t"
      "v_exp_f32 v48, v48\n\t"
      "v_mfma_f32_16x16x32_bf16 v[56:59], a[68:71], v[36:39], v[56:59]\n\t"
      "ds_write_b128 v218, v[100:103] offset:35840\n\t"
      "v_mfma_f32_16x16x32_bf16 v[56:59], a[72:75], v[40:43], v[56:59]\n\t"
      "v_exp_f32 v49, v49\n\t"
      "v_smfmac_f32_16x16x64_bf16 v[188:191], v[192:195], v[96:103], v196\n\t"
      "v_mfma_f32_16x16x32_bf16 v[56:59], a[76:79], v[44:47], v[56:59]\n\t"
      "v_mfma_f32_16x16x32_bf16 v[60:63], a[80:83], v[32:35], 0\n\t"
      "v_exp_f32 v50, v50\n\t"
      "v_mfma_f32_16x16x32_bf16 v[60:63], a[84:87], v[36:39], v[60:63]\n\t"
      "ds_read_b64_tr_b16 a[180:181], v249 offset:32768\n\t"
      "ds_read_b64_tr_b16 a[182:183], v249 offset:36864\n\t"
      "ds_read_b64_tr_b16 a[208:209], v250 offset:32768\n\t"
      "ds_read_b64_tr_b16 a[210:211], v250 offset:36864\n\t"
      "v_mfma_f32_16x16x32_bf16 v[60:63], a[88:91], v[40:43], v[60:63]\n\t"
      "ds_read_b64_tr_b16 a[212:213], v251 offset:32768\n\t"
      "ds_read_b64_tr_b16 a[214:215], v251 offset:36864\n\t"
      "ds_read_b64_tr_b16 a[240:241], v252 offset:32768\n\t"
      "ds_read_b64_tr_b16 a[242:243], v252 offset:36864\n\t"
      "v_exp_f32 v51, v51\n\t"
      "v_mfma_f32_16x16x32_bf16 v[60:63], a[92:95], v[44:47], v[60:63]\n\t"
      "ds_read_b64_tr_b16 a[244:245], v253 offset:32768\n\t"
      "ds_read_b64_tr_b16 a[246:247], v253 offset:36864\n\t"
      "s_waitcnt lgkmcnt(13)\n\t"
      "v_mfma_f32_16x16x32_bf16 v[144:147], a[144:147], v[88:91], v[144:147]\n\t"
      "v_exp_f32 v52, v52\n\t"
      "v_mfma_f32_16x16x32_bf16 v[148:151], a[148:151], v[88:91], v[148:151]\n\t"
      "s_add_u32 m0, s55, s56\n\t"
      "v_mfma_f32_16x16x32_bf16 v[152:155], a[144:147], v[104:107], v[152:155]\n\t"
      "buffer_load_dwordx4 v226, s[36:39], s54 offen nt lds\n\t"
      "v_exp_f32 v53, v53\n\t"
      "v_mfma_f32_16x16x32_bf16 v[156:159], a[148:151], v[104:107], v[156:159]\n\t"
      "v_cvt_pk_bf16_f32 v88, v48, v49\n\t"
      "v_cvt_pk_bf16_f32 v89, v50, v51\n\t"
      "s_waitcnt lgkmcnt(0)\n\t"
      "v_mfma_f32_16x16x32_bf16 v[144:147], a[176:179], v[112:115], v[144:147]\n\t"
      "v_exp_f32 v54, v54\n\t"
      "v_mfma_f32_16x16x32_bf16 v[148:151], a[180:183], v[112:115], v[148:151]\n\t"
      "v_cvt_pk_bf16_f32 v90, v52, v53\n\t"
      "s_add_u32 m0, s55, s57\n\t"
      "v_mfma_f32_16x16x32_bf16 v[152:155], a[176:179], v[116:119], v[152:155]\n\t"
      "buffer_load_dwordx4 v226, s[36:39], s54 offen offset:1024 nt lds\n\t"
      "v_exp_f32 v55, v55\n\t"
      "v_mfma_f32_16x16x32_bf16 v[156:159], a[180:183], v[116:119], v[156:159]\n\t"
      "v_cvt_pk_bf16_f32 v91, v54, v55\n\t"
      "s_add_u32 s55, s55, 8704\n\t"
      "s_cmp_eq_u32 s55, 26112\n\t"
      "s_cselect_b32 s55, 0, s55\n\t"
      "v_mfma_f32_16x16x32_bf16 v[144:147], a[208:211], v[120:123], v[144:147]\n\t"
      "v_exp_f32 v56, v56\n\t"
      "v_mfma_f32_16x16x32_bf16 v[148:151], a[212:215], v[120:123], v[148:151]\n\t"
      "ds_write_b128 v218, v[88:91] offset:36864\n\t"
      "v_mfma_f32_16x16x32_bf16 v[152:155], a[208:211], v[124:127], v[152:155]\n\t"
      "v_exp_f32 v57, v57\n\t"
      "v_mfma_f32_16x16x32_bf16 v[156:159], a[212:215], v[124:127], v[156:159]\n\t"
      "v_cvt_pk_bf16_f32 v104, v56, v57\n\t"
      "v_add_u32 v216, s55, v217\n\t"
      "v_mfma_f32_16x16x32_bf16 v[144:147], a[240:243], v[128:131], v[144:147]\n\t"
      "v_exp_f32 v58, v58\n\t"
      "v_mfma_f32_16x16x32_bf16 v[148:151], a[244:247], v[128:131], v[148:151]\n\t"
      "ds_read_b128 v[160:163], v219 offset:6144\n\t"
      "ds_read_b128 v[164:167], v219 offset:7168\n\t"
      "v_mfma_f32_16x16x32_bf16 v[152:155], a[240:243], v[132:135], v[152:155]\n\t"
      "v_exp_f32 v59, v59\n\t"
      "v_mfma_f32_16x16x32_bf16 v[156:159], a[244:247], v[132:135], v[156:159]\n\t"
      "ds_read_b128 v[168:171], v220 offset:6144\n\t"
      "ds_read_b128 v[172:175], v220 offset:7168\n\t"
      "v_cvt_pk_bf16_f32 v105, v58, v59\n\t"
      "v_mfma_f32_16x16x32_bf16 v[64:67], a[96:99], v[16:19], 0\n\t"
      "v_exp_f32 v60, v60\n\t"
      "v_mfma_f32_16x16x32_bf16 v[64:67], a[100:103], v[20:23], v[64:67]\n\t"
      "ds_read_b128 v[176:179], v221 offset:6144\n\t"
      "ds_read_b128 v[180:183], v221 offset:7168\n\t"
      "v_mfma_f32_16x16x32_bf16 v[64:67], a[104:107], v[24:27], v[64:67]\n\t"
      "v_exp_f32 v61, v61\n\t"
      "v_mfma_f32_16x16x32_bf16 v[64:67], a[108:111], v[28:31], v[64:67]\n\t"
      "v_cvt_pk_bf16_f32 v106, v60, v61\n\t"
      "v_mfma_f32_16x16x32_bf16 v[68:71], a[112:115], v[16:19], 0\n\t"
      "s_waitcnt vmcnt(2)\n\t"
      "s_barrier\n\t"
      "ds_read_b64_tr_b16 a[152:153], v246 offset:0\n\t"
      "ds_read_b64_tr_b16 a[154:155], v246 offset:4096\n\t"
      "v_exp_f32 v62, v62\n\t"
      "v_mfma_f32_16x16x32_bf16 v[68:71], a[116:119], v[20:23], v[68:71]\n\t"
      "ds_read_b64_tr_b16 a[156:157], v247 offset:0\n\t"
      "ds_read_b64_tr_b16 a[158:159], v247 offset:4096\n\t"
      "v_mfma_f32_16x16x32_bf16 v[68:71], a[120:123], v[24:27], v[68:71]\n\t"
      "ds_read_b64_tr_b16 a[184:185], v248 offset:0\n\t"
      "ds_read_b64_tr_b16 a[186:187], v248 offset:4096\n\t"
      "v_exp_f32 v63, v63\n\t"
      "v_mfma_f32_16x16x32_bf16 v[68:71], a[124:127], v[28:31], v[68:71]\n\t"
      "ds_read_b64_tr_b16 a[188:189], v249 offset:0\n\t"
      "ds_read_b64_tr_b16 a[190:191], v249 offset:4096\n\t"
      "v_cvt_pk_bf16_f32 v107, v62, v63\n\t"
      "v_mfma_f32_16x16x32_bf16 v[72:75], a[96:99], v[32:35], 0\n\t"
      "ds_read_b64_tr_b16 a[216:217], v250 offset:0\n\t"
      "ds_read_b64_tr_b16 a[218:219], v250 offset:4096\n\t"
      "v_exp_f32 v64, v64\n\t"
      "v_mfma_f32_16x16x32_bf16 v[72:75], a[100:103], v[36:39], v[72:75]\n\t"
      "ds_write_b128 v218, v[104:107] offset:37888\n\t"
      "v_mfma_f32_16x16x32_bf16 v[72:75], a[104:107], v[40:43], v[72:75]\n\t"
      "v_exp_f32 v65, v65\n\t"
      "v_mfma_f32_16x16x32_bf16 v[72:75], a[108:111], v[44:47], v[72:75]\n\t"
      "s_add_u32 s54, s54, 8192\n\t"
      "v_mfma_f32_16x16x32_bf16 v[76:79], a[112:115], v[32:35], 0\n\t"
      "v_exp_f32 v66, v66\n\t"
      "v_mfma_f32_16x16x32_bf16 v[76:79], a[116:119], v[36:39], v[76:79]\n\t"
      "ds_read_b64_tr_b16 a[220:221], v251 offset:0\n\t"
      "ds_read_b64_tr_b16 a[222:223], v251 offset:4096\n\t"
      "v_mfma_f32_16x16x32_bf16 v[76:79], a[120:123], v[40:43], v[76:79]\n\t"
      "ds_read_b64_tr_b16 a[248:249], v252 offset:0\n\t"
      "ds_read_b64_tr_b16 a[250:251], v252 offset:4096\n\t"
      "ds_read_b64_tr_b16 a[252:253], v253 offset:0\n\t"
      "ds_read_b64_tr_b16 a[254:255], v253 offset:4096\n\t"
      "v_exp_f32 v67, v67\n\t"
      "v_mfma_f32_16x16x32_bf16 v[76:79], a[124:127], v[44:47], v[76:79]\n\t"
      "ds_read_b128 v[16:19], v216 offset:0\n\t"
      "ds_read_b128 v[20:23], v216 offset:64\n\t"
      "s_waitcnt lgkmcnt(15)\n\t"
      "v_mfma_f32_16x16x32_bf16 v[144:147], a[152:155], v[92:95], v[144:147]\n\t"
      "v_exp_f32 v68, v68\n\t"
      "v_mfma_f32_16x16x32_bf16 v[148:151], a[156:159], v[92:95], v[148:151]\n\t"
      "ds_read_b128 v[24:27], v216 offset:128\n\t"
      "ds_read_b128 v[28:31], v216 offset:192\n\t"
      "v_mfma_f32_16x16x32_bf16 v[152:155], a[152:155], v[108:111], v[152:155]\n\t"
      "v_exp_f32 v69, v69\n\t"
      "v_mfma_f32_16x16x32_bf16 v[156:159], a[156:159], v[108:111], v[156:159]\n\t"
      "ds_read_b128 v[32:35], v216 offset:4352\n\t"
      "ds_read_b128 v[36:39], v216 offset:4416\n\t"
      "v_cvt_pk_bf16_f32 v92, v64, v65\n\t"
      "v_cvt_pk_bf16_f32 v93, v66, v67\n\t"
      "s_waitcnt lgkmcnt(6)\n\t"
      "v_mfma_f32_16x16x32_bf16 v[144:147], a[184:187], v[160:163], v[144:147]\n\t"
      "v_exp_f32 v70, v70\n\t"
      "v_mfma_f32_16x16x32_bf16 v[148:151], a[188:191], v[160:163], v[148:151]\n\t"
      "ds_read_b128 v[40:43], v216 offset:4480\n\t"
      "ds_read_b128 v[44:47], v216 offset:4544\n\t"
      "v_cvt_pk_bf16_f32 v94, v68, v69\n\t"
      "v_mfma_f32_16x16x32_bf16 v[152:155], a[184:187], v[164:167], v[152:155]\n\t"
      "v_exp_f32 v71, v71\n\t"
      "v_mfma_f32_16x16x32_bf16 v[156:159], a[188:191], v[164:167], v[156:159]\n\t"
      "v_cvt_pk_bf16_f32 v95, v70, v71\n\t"
      "v_mfma_f32_16x16x32_bf16 v[144:147], a[216:219], v[168:171], v[144:147]\n\t"
      "v_exp_f32 v72, v72\n\t"
      "v_mfma_f32_16x16x32_bf16 v[148:151], a[220:223], v[168:171], v[148:151]\n\t"
      "ds_write_b128 v218, v[92:95] offset:38912\n\t"
      "v_mfma_f32_16x16x32_bf16 v[152:155], a[216:219], v[172:175], v[152:155]\n\t"
      "v_exp_f32 v73, v73\n\t"
      "v_smfmac_f32_16x16x64_bf16 v[184:187], v[192:195], v[88:95], v196\n\t"
      "v_mfma_f32_16x16x32_bf16 v[156:159], a[220:223], v[172:175], v[156:159]\n\t"
      "v_cvt_pk_bf16_f32 v108, v72, v73\n\t"
      "v_mfma_f32_16x16x32_bf16 v[144:147], a[248:251], v[176:179], v[144:147]\n\t"
      "v_exp_f32 v74, v74\n\t"
      "v_mfma_f32_16x16x32_bf16 v[148:151], a[252:255], v[176:179], v[148:151]\n\t"
      "v_mfma_f32_16x16x32_bf16 v[152:155], a[248:251], v[180:183], v[152:155]\n\t"
      "v_exp_f32 v75, v75\n\t"
      "v_add_f32 v201, v184, v185\n\t"
      "v_mfma_f32_16x16x32_bf16 v[156:159], a[252:255], v[180:183], v[156:159]\n\t"
      "v_cvt_pk_bf16_f32 v109, v74, v75\n\t"
      "v_mov_b32 v184, 0\n\t"
      "v_mov_b32 v185, 0\n\t"
      "s_waitcnt lgkmcnt(1)\n\t"
      ".p2align 6\n\t"
      "LOOP_%=:\n\t"
      "v_mfma_f32_16x16x32_bf16 v[48:51], a[0:3], v[16:19], 0\n\t"
      "v_exp_f32 v76, v76\n\t"
      "v_mfma_f32_16x16x32_bf16 v[48:51], a[4:7], v[20:23], v[48:51]\n\t"
      "v_mfma_f32_16x16x32_bf16 v[48:51], a[8:11], v[24:27], v[48:51]\n\t"
      "v_exp_f32 v77, v77\n\t"
      "v_mfma_f32_16x16x32_bf16 v[48:51], a[12:15], v[28:31], v[48:51]\n\t"
      "v_cvt_pk_bf16_f32 v110, v76, v77\n\t"
      "v_mul_f32 v163, v147, v197\n\t"
      "v_mfma_f32_16x16x32_bf16 v[52:55], a[16:19], v[16:19], 0\n\t"
      "v_exp_f32 v78, v78\n\t"
      "v_mfma_f32_16x16x32_bf16 v[52:55], a[20:23], v[20:23], v[52:55]\n\t"
      "v_mul_f32 v160, v144, v197\n\t"
      "v_mul_f32 v164, v148, v197\n\t"
      "v_mul_f32 v168, v152, v198\n\t"
      "v_mfma_f32_16x16x32_bf16 v[52:55], a[24:27], v[24:27], v[52:55]\n\t"
      "v_exp_f32 v79, v79\n\t"
      "v_mul_f32 v169, v153, v198\n\t"
      "v_mfma_f32_16x16x32_bf16 v[52:55], a[28:31], v[28:31], v[52:55]\n\t"
      "v_cvt_pk_bf16_f32 v111, v78, v79\n\t"
      "v_mul_f32 v165, v149, v197\n\t"
      "v_mul_f32 v170, v154, v198\n\t"
      "v_mfma_f32_16x16x32_bf16 v[56:59], a[0:3], v[32:35], 0\n\t"
      "v_exp_f32 v48, v48\n\t"
      "v_mul_f32 v171, v155, v198\n\t"
      "v_mfma_f32_16x16x32_bf16 v[56:59], a[4:7], v[36:39], v[56:59]\n\t"
      "ds_write_b128 v218, v[108:111] offset:39936\n\t"
      "v_mfma_f32_16x16x32_bf16 v[56:59], a[8:11], v[40:43], v[56:59]\n\t"
      "v_exp_f32 v49, v49\n\t"
      "v_mul_f32 v172, v156, v198\n\t"
      "v_smfmac_f32_16x16x64_bf16 v[188:191], v[192:195], v[104:111], v196\n\t"
      "v_mul_f32 v161, v145, v197\n\t"
      "v_mul_f32 v166, v150, v197\n\t"
      "v_mul_f32 v173, v157, v198\n\t"
      "v_mfma_f32_16x16x32_bf16 v[56:59], a[12:15], v[44:47], v[56:59]\n\t"
      "v_mul_f32 v162, v146, v197\n\t"
      "v_mul_f32 v167, v151, v197\n\t"
      "v_mul_f32 v174, v158, v198\n\t"
      "v_mfma_f32_16x16x32_bf16 v[60:63], a[16:19], v[32:35], 0\n\t"
      "v_exp_f32 v50, v50\n\t"
      "v_mul_f32 v175, v159, v198\n\t"
      "v_mfma_f32_16x16x32_bf16 v[60:63], a[20:23], v[36:39], v[60:63]\n\t"
      "v_add_f32 v202, v188, v189\n\t"
      "v_cndmask_b32 v201, v201, v202, s[60:61]\n\t"
      "v_mfma_f32_16x16x32_bf16 v[60:63], a[24:27], v[40:43], v[60:63]\n\t"
      "v_exp_f32 v51, v51\n\t"
      "v_mfma_f32_16x16x32_bf16 v[60:63], a[28:31], v[44:47], v[60:63]\n\t"
      "ds_write_b32 v224, v201 offset:512\n\t"
      "v_mfma_f32_16x16x32_bf16 v[144:147], a[128:131], v[80:83], 0\n\t"
      "v_exp_f32 v52, v52\n\t"
      "v_mfma_f32_16x16x32_bf16 v[148:151], a[132:135], v[80:83], 0\n\t"
      "v_mov_b32 v188, 0\n\t"
      "v_mov_b32 v189, 0\n\t"
      "v_mfma_f32_16x16x32_bf16 v[152:155], a[128:131], v[96:99], 0\n\t"
      "v_exp_f32 v53, v53\n\t"
      "v_mfma_f32_16x16x32_bf16 v[156:159], a[132:135], v[96:99], 0\n\t"
      "v_cvt_pk_bf16_f32 v80, v48, v49\n\t"
      "v_cvt_pk_bf16_f32 v179, v166, v167\n\t"
      "v_mfma_f32_16x16x32_bf16 v[144:147], a[136:139], v[84:87], v[144:147]\n\t"
      "v_exp_f32 v54, v54\n\t"
      "v_mfma_f32_16x16x32_bf16 v[148:151], a[140:143], v[84:87], v[148:151]\n\t"
      "v_cvt_pk_bf16_f32 v81, v50, v51\n\t"
      "v_cvt_pk_bf16_f32 v82, v52, v53\n\t"
      "v_mfma_f32_16x16x32_bf16 v[152:155], a[136:139], v[100:103], v[152:155]\n\t"
      "v_exp_f32 v55, v55\n\t"
      "v_mfma_f32_16x16x32_bf16 v[156:159], a[140:143], v[100:103], v[156:159]\n\t"
      "v_cvt_pk_bf16_f32 v83, v54, v55\n\t"
      "s_waitcnt vmcnt(6) lgkmcnt(0)\n\t"
      "s_barrier\n\t"
      "v_mfma_f32_16x16x32_bf16 v[144:147], a[144:147], v[88:91], v[144:147]\n\t"
      "ds_read_b128 v[204:207], v225 offset:512\n\t"
      "ds_read_b128 v[112:115], v219 offset:32768\n\t"
      "v_exp_f32 v56, v56\n\t"
      "v_mfma_f32_16x16x32_bf16 v[148:151], a[148:151], v[88:91], v[148:151]\n\t"
      "ds_read_b128 v[116:119], v219 offset:33792\n\t"
      "ds_read_b128 v[120:123], v219 offset:34816\n\t"
      "v_mfma_f32_16x16x32_bf16 v[152:155], a[144:147], v[104:107], v[152:155]\n\t"
      "v_exp_f32 v57, v57\n\t"
      "v_mfma_f32_16x16x32_bf16 v[156:159], a[148:151], v[104:107], v[156:159]\n\t"
      "ds_read_b128 v[124:127], v219 offset:35840\n\t"
      "ds_read_b128 v[128:131], v219 offset:36864\n\t"
      "v_cvt_pk_bf16_f32 v96, v56, v57\n\t"
      "v_mfma_f32_16x16x32_bf16 v[144:147], a[152:155], v[92:95], v[144:147]\n\t"
      "v_exp_f32 v58, v58\n\t"
      "v_mfma_f32_16x16x32_bf16 v[148:151], a[156:159], v[92:95], v[148:151]\n\t"
      "ds_read_b128 v[132:135], v219 offset:37888\n\t"
      "ds_read_b128 v[136:139], v219 offset:38912\n\t"
      "v_mfma_f32_16x16x32_bf16 v[152:155], a[152:155], v[108:111], v[152:155]\n\t"
      "v_exp_f32 v59, v59\n\t"
      "v_mfma_f32_16x16x32_bf16 v[156:159], a[156:159], v[108:111], v[156:159]\n\t"
      "ds_read_b128 v[140:143], v219 offset:39936\n\t"
      "ds_read_b128 v[208:211], v223 offset:16384\n\t"
      "v_cvt_pk_bf16_f32 v97, v58, v59\n\t"
      "v_mfma_f32_16x16x32_bf16 v[64:67], a[32:35], v[16:19], 0\n\t"
      "v_exp_f32 v60, v60\n\t"
      "v_mfma_f32_16x16x32_bf16 v[64:67], a[36:39], v[20:23], v[64:67]\n\t"
      "ds_write_b128 v218, v[80:83] offset:0\n\t"
      "v_mfma_f32_16x16x32_bf16 v[64:67], a[40:43], v[24:27], v[64:67]\n\t"
      "v_exp_f32 v61, v61\n\t"
      "v_mfma_f32_16x16x32_bf16 v[64:67], a[44:47], v[28:31], v[64:67]\n\t"
      "v_cvt_pk_bf16_f32 v98, v60, v61\n\t"
      "s_waitcnt lgkmcnt(10)\n\t"
      "v_add_f32 v204, v204, v205\n\t"
      "v_add_f32 v206, v206, v207\n\t"
      "v_mfma_f32_16x16x32_bf16 v[68:71], a[48:51], v[16:19], 0\n\t"
      "v_exp_f32 v62, v62\n\t"
      "v_mfma_f32_16x16x32_bf16 v[68:71], a[52:55], v[20:23], v[68:71]\n\t"
      "ds_read_b128 v[212:215], v223 offset:17472\n\t"
      "v_cvt_pk_bf16_f32 v180, v168, v169\n\t"
      "v_mfma_f32_16x16x32_bf16 v[68:71], a[56:59], v[24:27], v[68:71]\n\t"
      "v_exp_f32 v63, v63\n\t"
      "v_mfma_f32_16x16x32_bf16 v[68:71], a[60:63], v[28:31], v[68:71]\n\t"
      "v_cvt_pk_bf16_f32 v99, v62, v63\n\t"
      "v_add_f32 v204, v204, v206\n\t"
      "v_mfma_f32_16x16x32_bf16 v[72:75], a[32:35], v[32:35], 0\n\t"
      "v_exp_f32 v64, v64\n\t"
      "v_mfma_f32_16x16x32_bf16 v[72:75], a[36:39], v[36:39], v[72:75]\n\t"
      "ds_write_b128 v218, v[96:99] offset:1024\n\t"
      "v_mfma_f32_16x16x32_bf16 v[72:75], a[40:43], v[40:43], v[72:75]\n\t"
      "v_exp_f32 v65, v65\n\t"
      "v_mfma_f32_16x16x32_bf16 v[72:75], a[44:47], v[44:47], v[72:75]\n\t"
      "v_cvt_pk_bf16_f32 v84, v64, v65\n\t"
      "v_rcp_f32 v197, v204\n\t"
      "v_mfma_f32_16x16x32_bf16 v[76:79], a[48:51], v[32:35], 0\n\t"
      "v_exp_f32 v66, v66\n\t"
      "v_mfma_f32_16x16x32_bf16 v[76:79], a[52:55], v[36:39], v[76:79]\n\t"
      "v_cvt_pk_bf16_f32 v176, v160, v161\n\t"
      "v_cvt_pk_bf16_f32 v181, v170, v171\n\t"
      "v_mfma_f32_16x16x32_bf16 v[76:79], a[56:59], v[40:43], v[76:79]\n\t"
      "v_exp_f32 v67, v67\n\t"
      "v_mfma_f32_16x16x32_bf16 v[76:79], a[60:63], v[44:47], v[76:79]\n\t"
      "v_cvt_pk_bf16_f32 v85, v66, v67\n\t"
      "s_waitcnt lgkmcnt(4)\n\t"
      "v_mov_b32 v198, v197\n\t"
      "v_max3_u32 v200, v200, v204, v197\n\t"
      "v_mfma_f32_16x16x32_bf16 v[144:147], a[160:163], v[112:115], v[144:147]\n\t"
      "v_exp_f32 v68, v68\n\t"
      "v_mfma_f32_16x16x32_bf16 v[148:151], a[164:167], v[112:115], v[148:151]\n\t"
      "v_cvt_pk_bf16_f32 v177, v162, v163\n\t"
      "v_cvt_pk_bf16_f32 v182, v172, v173\n\t"
      "v_mfma_f32_16x16x32_bf16 v[152:155], a[160:163], v[116:119], v[152:155]\n\t"
      "v_exp_f32 v69, v69\n\t"
      "v_mfma_f32_16x16x32_bf16 v[156:159], a[164:167], v[116:119], v[156:159]\n\t"
      "v_cvt_pk_bf16_f32 v86, v68, v69\n\t"
      "v_permlane16_swap_b32 v197, v198\n\t"
      "v_mfma_f32_16x16x32_bf16 v[144:147], a[168:171], v[120:123], v[144:147]\n\t"
      "v_exp_f32 v70, v70\n\t"
      "v_mfma_f32_16x16x32_bf16 v[148:151], a[172:175], v[120:123], v[148:151]\n\t"
      "ds_read_b128 v[112:115], v220 offset:32768\n\t"
      "ds_read_b128 v[116:119], v220 offset:33792\n\t"
      "v_mfma_f32_16x16x32_bf16 v[152:155], a[168:171], v[124:127], v[152:155]\n\t"
      "v_exp_f32 v71, v71\n\t"
      "v_mfma_f32_16x16x32_bf16 v[156:159], a[172:175], v[124:127], v[156:159]\n\t"
      "v_cvt_pk_bf16_f32 v87, v70, v71\n\t"
      "v_cvt_pk_bf16_f32 v183, v174, v175\n\t"
      "v_mfma_f32_16x16x32_bf16 v[144:147], a[176:179], v[128:131], v[144:147]\n\t"
      "v_exp_f32 v72, v72\n\t"
      "s_add_u32 m0, s55, s56\n\t"
      "v_mfma_f32_16x16x32_bf16 v[148:151], a[180:183], v[128:131], v[148:151]\n\t"
      "ds_write_b128 v218, v[84:87] offset:2048\n\t"
      "v_mfma_f32_16x16x32_bf16 v[152:155], a[176:179], v[132:135], v[152:155]\n\t"
      "v_exp_f32 v73, v73\n\t"
      "v_smfmac_f32_16x16x64_bf16 v[184:187], v[192:195], v[80:87], v196\n\t"
      "v_cvt_pk_bf16_f32 v178, v164, v165\n\t"
      "v_mfma_f32_16x16x32_bf16 v[156:159], a[180:183], v[132:135], v[156:159]\n\t"
      "buffer_load_dwordx4 v226, s[36:39], s54 offen nt lds\n\t"
      "v_cvt_pk_bf16_f32 v100, v72, v73\n\t"
      "v_mfma_f32_16x16x32_bf16 v[144:147], a[184:187], v[136:139], v[144:147]\n\t"
      "v_exp_f32 v74, v74\n\t"
      "v_mfma_f32_16x16x32_bf16 v[148:151], a[188:191], v[136:139], v[148:151]\n\t"
      "ds_read_b128 v[120:123], v220 offset:34816\n\t"
      "ds_read_b128 v[124:127], v220 offset:35840\n\t"
      "v_mfma_f32_16x16x32_bf16 v[152:155], a[184:187], v[140:143], v[152:155]\n\t"
      "v_exp_f32 v75, v75\n\t"
      "v_mfma_f32_16x16x32_bf16 v[156:159], a[188:191], v[140:143], v[156:159]\n\t"
      "ds_read_b128 v[128:131], v220 offset:36864\n\t"
      "ds_read_b128 v[132:135], v220 offset:37888\n\t"
      "v_cvt_pk_bf16_f32 v101, v74, v75\n\t"
      "v_mfma_f32_16x16x32_bf16 v[48:51], a[64:67], v[16:19], 0\n\t"
      "v_exp_f32 v76, v76\n\t"
      "v_mfma_f32_16x16x32_bf16 v[48:51], a[68:71], v[20:23], v[48:51]\n\t"
      "ds_read_b128 v[136:139], v220 offset:38912\n\t"
      "ds_read_b128 v[140:143], v220 offset:39936\n\t"
      "v_mfma_f32_16x16x32_bf16 v[48:51], a[72:75], v[24:27], v[48:51]\n\t"
      "v_exp_f32 v77, v77\n\t"
      "s_add_u32 m0, s55, s57\n\t"
      "v_mfma_f32_16x16x32_bf16 v[48:51], a[76:79], v[28:31], v[48:51]\n\t"
      "buffer_load_dwordx4 v226, s[36:39], s54 offen offset:1024 nt lds\n\t"
      "v_cvt_pk_bf16_f32 v102, v76, v77\n\t"
      "v_mfma_f32_16x16x32_bf16 v[52:55], a[80:83], v[16:19], 0\n\t"
      "v_exp_f32 v78, v78\n\t"
      "v_mfma_f32_16x16x32_bf16 v[52:55], a[84:87], v[20:23], v[52:55]\n\t"
      "ds_write_b64 v222, v[176:177] offset:0\n\t"
      "v_mfma_f32_16x16x32_bf16 v[52:55], a[88:91], v[24:27], v[52:55]\n\t"
      "v_exp_f32 v79, v79\n\t"
      "v_mfma_f32_16x16x32_bf16 v[52:55], a[92:95], v[28:31], v[52:55]\n\t"
      "v_cvt_pk_bf16_f32 v103, v78, v79\n\t"
      "s_waitcnt lgkmcnt(11)\n\t"
      "v_mfma_f32_16x16x32_bf16 v[56:59], a[64:67], v[32:35], 0\n\t"
      "v_exp_f32 v48, v48\n\t"
      "v_mfma_f32_16x16x32_bf16 v[56:59], a[68:71], v[36:39], v[56:59]\n\t"
      "ds_write_b128 v218, v[100:103] offset:3072\n\t"
      "v_mfma_f32_16x16x32_bf16 v[56:59], a[72:75], v[40:43], v[56:59]\n\t"
      "v_exp_f32 v49, v49\n\t"
      "v_smfmac_f32_16x16x64_bf16 v[188:191], v[192:195], v[96:103], v196\n\t"
      "s_add_u32 s55, s55, 8704\n\t"
      "s_cmp_eq_u32 s55, 26112\n\t"
      "s_cselect_b32 s55, 0, s55\n\t"
      "v_mfma_f32_16x16x32_bf16 v[56:59], a[76:79], v[44:47], v[56:59]\n\t"
      "buffer_store_dwordx4 v[208:211], v227, s[48:51], s58 offen sc1\n\t"
      "v_cvt_pk_bf16_f32 v88, v48, v49\n\t"
      "v_mfma_f32_16x16x32_bf16 v[60:63], a[80:83], v[32:35], 0\n\t"
      "v_exp_f32 v50, v50\n\t"
      "v_mfma_f32_16x16x32_bf16 v[60:63], a[84:87], v[36:39], v[60:63]\n\t"
      "ds_write_b64 v222, v[178:179] offset:32\n\t"
      "v_mfma_f32_16x16x32_bf16 v[60:63], a[88:91], v[40:43], v[60:63]\n\t"
      "v_exp_f32 v51, v51\n\t"
      "v_mfma_f32_16x16x32_bf16 v[60:63], a[92:95], v[44:47], v[60:63]\n\t"
      "v_cvt_pk_bf16_f32 v89, v50, v51\n\t"
      "s_waitcnt lgkmcnt(3)\n\t"
      "v_mfma_f32_16x16x32_bf16 v[144:147], a[192:195], v[112:115], v[144:147]\n\t"
      "v_exp_f32 v52, v52\n\t"
      "v_mfma_f32_16x16x32_bf16 v[148:151], a[196:199], v[112:115], v[148:151]\n\t"
      "ds_write_b64 v222, v[180:181] offset:4352\n\t"
      "v_mfma_f32_16x16x32_bf16 v[152:155], a[192:195], v[116:119], v[152:155]\n\t"
      "v_exp_f32 v53, v53\n\t"
      "v_mfma_f32_16x16x32_bf16 v[156:159], a[196:199], v[116:119], v[156:159]\n\t"
      "buffer_store_dwordx4 v[212:215], v227, s[48:51], s58 offen offset:1024 sc1\n\t"
      "v_cvt_pk_bf16_f32 v90, v52, v53\n\t"
      "v_mfma_f32_16x16x32_bf16 v[144:147], a[200:203], v[120:123], v[144:147]\n\t"
      "v_exp_f32 v54, v54\n\t"
      "v_mfma_f32_16x16x32_bf16 v[148:151], a[204:207], v[120:123], v[148:151]\n\t"
      "ds_read_b128 v[112:115], v221 offset:32768\n\t"
      "ds_read_b128 v[116:119], v221 offset:33792\n\t"
      "v_mfma_f32_16x16x32_bf16 v[152:155], a[200:203], v[124:127], v[152:155]\n\t"
      "v_exp_f32 v55, v55\n\t"
      "v_mfma_f32_16x16x32_bf16 v[156:159], a[204:207], v[124:127], v[156:159]\n\t"
      "v_cvt_pk_bf16_f32 v91, v54, v55\n\t"
      "v_add_u32 v216, s55, v217\n\t"
      "v_mfma_f32_16x16x32_bf16 v[144:147], a[208:211], v[128:131], v[144:147]\n\t"
      "v_exp_f32 v56, v56\n\t"
      "v_mfma_f32_16x16x32_bf16 v[148:151], a[212:215], v[128:131], v[148:151]\n\t"
      "ds_write_b128 v218, v[88:91] offset:4096\n\t"
      "v_mfma_f32_16x16x32_bf16 v[152:155], a[208:211], v[132:135], v[152:155]\n\t"
      "v_exp_f32 v57, v57\n\t"
      "v_mfma_f32_16x16x32_bf16 v[156:159], a[212:215], v[132:135], v[156:159]\n\t"
      "v_cvt_pk_bf16_f32 v104, v56, v57\n\t"
      "s_mov_b32 s50, 98304\n\t"
      "v_mfma_f32_16x16x32_bf16 v[144:147], a[216:219], v[136:139], v[144:147]\n\t"
      "v_exp_f32 v58, v58\n\t"
      "v_mfma_f32_16x16x32_bf16 v[148:151], a[220:223], v[136:139], v[148:151]\n\t"
      "ds_read_b128 v[120:123], v221 offset:34816\n\t"
      "ds_read_b128 v[124:127], v221 offset:35840\n\t"
      "v_mfma_f32_16x16x32_bf16 v[152:155], a[216:219], v[140:143], v[152:155]\n\t"
      "v_exp_f32 v59, v59\n\t"
      "v_mfma_f32_16x16x32_bf16 v[156:159], a[220:223], v[140:143], v[156:159]\n\t"
      "ds_read_b128 v[128:131], v221 offset:36864\n\t"
      "ds_read_b128 v[132:135], v221 offset:37888\n\t"
      "v_cvt_pk_bf16_f32 v105, v58, v59\n\t"
      "v_mfma_f32_16x16x32_bf16 v[64:67], a[96:99], v[16:19], 0\n\t"
      "v_exp_f32 v60, v60\n\t"
      "v_mfma_f32_16x16x32_bf16 v[64:67], a[100:103], v[20:23], v[64:67]\n\t"
      "ds_read_b128 v[136:139], v221 offset:38912\n\t"
      "ds_read_b128 v[140:143], v221 offset:39936\n\t"
      "v_mfma_f32_16x16x32_bf16 v[64:67], a[104:107], v[24:27], v[64:67]\n\t"
      "v_exp_f32 v61, v61\n\t"
      "v_mfma_f32_16x16x32_bf16 v[64:67], a[108:111], v[28:31], v[64:67]\n\t"
      "v_cvt_pk_bf16_f32 v106, v60, v61\n\t"
      "v_mfma_f32_16x16x32_bf16 v[68:71], a[112:115], v[16:19], 0\n\t"
      "v_exp_f32 v62, v62\n\t"
      "v_mfma_f32_16x16x32_bf16 v[68:71], a[116:119], v[20:23], v[68:71]\n\t"
      "ds_write_b64 v222, v[182:183] offset:4384\n\t"
      "v_mfma_f32_16x16x32_bf16 v[68:71], a[120:123], v[24:27], v[68:71]\n\t"
      "v_exp_f32 v63, v63\n\t"
      "v_mfma_f32_16x16x32_bf16 v[68:71], a[124:127], v[28:31], v[68:71]\n\t"
      "v_cvt_pk_bf16_f32 v107, v62, v63\n\t"
      "v_mfma_f32_16x16x32_bf16 v[72:75], a[96:99], v[32:35], 0\n\t"
      "v_exp_f32 v64, v64\n\t"
      "v_mfma_f32_16x16x32_bf16 v[72:75], a[100:103], v[36:39], v[72:75]\n\t"
      "ds_write_b128 v218, v[104:107] offset:5120\n\t"
      "v_mfma_f32_16x16x32_bf16 v[72:75], a[104:107], v[40:43], v[72:75]\n\t"
      "v_exp_f32 v65, v65\n\t"
      "v_mfma_f32_16x16x32_bf16 v[72:75], a[108:111], v[44:47], v[72:75]\n\t"
      "v_cvt_pk_bf16_f32 v92, v64, v65\n\t"
      "v_mfma_f32_16x16x32_bf16 v[76:79], a[112:115], v[32:35], 0\n\t"
      "v_exp_f32 v66, v66\n\t"
      "v_mfma_f32_16x16x32_bf16 v[76:79], a[116:119], v[36:39], v[76:79]\n\t"
      "ds_read_b128 v[16:19], v216 offset:0\n\t"
      "ds_read_b128 v[20:23], v216 offset:64\n\t"
      "v_mfma_f32_16x16x32_bf16 v[76:79], a[120:123], v[40:43], v[76:79]\n\t"
      "v_exp_f32 v67, v67\n\t"
      "v_mfma_f32_16x16x32_bf16 v[76:79], a[124:127], v[44:47], v[76:79]\n\t"
      "ds_read_b128 v[24:27], v216 offset:128\n\t"
      "ds_read_b128 v[28:31], v216 offset:192\n\t"
      "v_cvt_pk_bf16_f32 v93, v66, v67\n\t"
      "s_waitcnt lgkmcnt(6)\n\t"
      "v_mfma_f32_16x16x32_bf16 v[144:147], a[224:227], v[112:115], v[144:147]\n\t"
      "v_exp_f32 v68, v68\n\t"
      "v_mfma_f32_16x16x32_bf16 v[148:151], a[228:231], v[112:115], v[148:151]\n\t"
      "ds_read_b128 v[32:35], v216 offset:4352\n\t"
      "ds_read_b128 v[36:39], v216 offset:4416\n\t"
      "v_mfma_f32_16x16x32_bf16 v[152:155], a[224:227], v[116:119], v[152:155]\n\t"
      "v_exp_f32 v69, v69\n\t"
      "v_mfma_f32_16x16x32_bf16 v[156:159], a[228:231], v[116:119], v[156:159]\n\t"
      "v_cvt_pk_bf16_f32 v94, v68, v69\n\t"
      "v_mfma_f32_16x16x32_bf16 v[144:147], a[232:235], v[120:123], v[144:147]\n\t"
      "v_exp_f32 v70, v70\n\t"
      "v_mfma_f32_16x16x32_bf16 v[148:151], a[236:239], v[120:123], v[148:151]\n\t"
      "ds_read_b128 v[40:43], v216 offset:4480\n\t"
      "ds_read_b128 v[44:47], v216 offset:4544\n\t"
      "v_mfma_f32_16x16x32_bf16 v[152:155], a[232:235], v[124:127], v[152:155]\n\t"
      "v_exp_f32 v71, v71\n\t"
      "v_mfma_f32_16x16x32_bf16 v[156:159], a[236:239], v[124:127], v[156:159]\n\t"
      "v_cvt_pk_bf16_f32 v95, v70, v71\n\t"
      "v_mfma_f32_16x16x32_bf16 v[144:147], a[240:243], v[128:131], v[144:147]\n\t"
      "v_exp_f32 v72, v72\n\t"
      "v_mfma_f32_16x16x32_bf16 v[148:151], a[244:247], v[128:131], v[148:151]\n\t"
      "ds_write_b128 v218, v[92:95] offset:6144\n\t"
      "v_mfma_f32_16x16x32_bf16 v[152:155], a[240:243], v[132:135], v[152:155]\n\t"
      "v_exp_f32 v73, v73\n\t"
      "v_smfmac_f32_16x16x64_bf16 v[184:187], v[192:195], v[88:95], v196\n\t"
      "s_add_u32 s54, s54, 8192\n\t"
      "v_mfma_f32_16x16x32_bf16 v[156:159], a[244:247], v[132:135], v[156:159]\n\t"
      "v_cvt_pk_bf16_f32 v108, v72, v73\n\t"
      "v_mfma_f32_16x16x32_bf16 v[144:147], a[248:251], v[136:139], v[144:147]\n\t"
      "v_exp_f32 v74, v74\n\t"
      "v_mfma_f32_16x16x32_bf16 v[148:151], a[252:255], v[136:139], v[148:151]\n\t"
      "s_add_u32 s58, s58, 8192\n\t"
      "v_mfma_f32_16x16x32_bf16 v[152:155], a[248:251], v[140:143], v[152:155]\n\t"
      "v_exp_f32 v75, v75\n\t"
      "v_add_f32 v201, v184, v185\n\t"
      "v_mfma_f32_16x16x32_bf16 v[156:159], a[252:255], v[140:143], v[156:159]\n\t"
      "v_cvt_pk_bf16_f32 v109, v74, v75\n\t"
      "v_mov_b32 v184, 0\n\t"
      "v_mov_b32 v185, 0\n\t"
      "s_waitcnt lgkmcnt(1)\n\t"
      "v_mfma_f32_16x16x32_bf16 v[48:51], a[0:3], v[16:19], 0\n\t"
      "v_exp_f32 v76, v76\n\t"
      "v_mfma_f32_16x16x32_bf16 v[48:51], a[4:7], v[20:23], v[48:51]\n\t"
      "v_mfma_f32_16x16x32_bf16 v[48:51], a[8:11], v[24:27], v[48:51]\n\t"
      "v_exp_f32 v77, v77\n\t"
      "v_mfma_f32_16x16x32_bf16 v[48:51], a[12:15], v[28:31], v[48:51]\n\t"
      "v_cvt_pk_bf16_f32 v110, v76, v77\n\t"
      "v_mul_f32 v163, v147, v197\n\t"
      "v_mfma_f32_16x16x32_bf16 v[52:55], a[16:19], v[16:19], 0\n\t"
      "v_exp_f32 v78, v78\n\t"
      "v_mfma_f32_16x16x32_bf16 v[52:55], a[20:23], v[20:23], v[52:55]\n\t"
      "v_mul_f32 v160, v144, v197\n\t"
      "v_mul_f32 v164, v148, v197\n\t"
      "v_mul_f32 v168, v152, v198\n\t"
      "v_mfma_f32_16x16x32_bf16 v[52:55], a[24:27], v[24:27], v[52:55]\n\t"
      "v_exp_f32 v79, v79\n\t"
      "v_mul_f32 v169, v153, v198\n\t"
      "v_mfma_f32_16x16x32_bf16 v[52:55], a[28:31], v[28:31], v[52:55]\n\t"
      "v_cvt_pk_bf16_f32 v111, v78, v79\n\t"
      "v_mul_f32 v165, v149, v197\n\t"
      "v_mul_f32 v170, v154, v198\n\t"
      "v_mfma_f32_16x16x32_bf16 v[56:59], a[0:3], v[32:35], 0\n\t"
      "v_exp_f32 v48, v48\n\t"
      "v_mul_f32 v171, v155, v198\n\t"
      "v_mfma_f32_16x16x32_bf16 v[56:59], a[4:7], v[36:39], v[56:59]\n\t"
      "ds_write_b128 v218, v[108:111] offset:7168\n\t"
      "v_mfma_f32_16x16x32_bf16 v[56:59], a[8:11], v[40:43], v[56:59]\n\t"
      "v_exp_f32 v49, v49\n\t"
      "v_mul_f32 v172, v156, v198\n\t"
      "v_smfmac_f32_16x16x64_bf16 v[188:191], v[192:195], v[104:111], v196\n\t"
      "v_mul_f32 v161, v145, v197\n\t"
      "v_mul_f32 v166, v150, v197\n\t"
      "v_mul_f32 v173, v157, v198\n\t"
      "v_mfma_f32_16x16x32_bf16 v[56:59], a[12:15], v[44:47], v[56:59]\n\t"
      "v_mul_f32 v162, v146, v197\n\t"
      "v_mul_f32 v167, v151, v197\n\t"
      "v_mul_f32 v174, v158, v198\n\t"
      "v_mfma_f32_16x16x32_bf16 v[60:63], a[16:19], v[32:35], 0\n\t"
      "v_exp_f32 v50, v50\n\t"
      "v_mul_f32 v175, v159, v198\n\t"
      "v_mfma_f32_16x16x32_bf16 v[60:63], a[20:23], v[36:39], v[60:63]\n\t"
      "v_add_f32 v202, v188, v189\n\t"
      "v_cndmask_b32 v201, v201, v202, s[60:61]\n\t"
      "v_mfma_f32_16x16x32_bf16 v[60:63], a[24:27], v[40:43], v[60:63]\n\t"
      "v_exp_f32 v51, v51\n\t"
      "v_mfma_f32_16x16x32_bf16 v[60:63], a[28:31], v[44:47], v[60:63]\n\t"
      "ds_write_b32 v224, v201 offset:0\n\t"
      "v_mfma_f32_16x16x32_bf16 v[144:147], a[128:131], v[80:83], 0\n\t"
      "v_exp_f32 v52, v52\n\t"
      "v_mfma_f32_16x16x32_bf16 v[148:151], a[132:135], v[80:83], 0\n\t"
      "v_mov_b32 v188, 0\n\t"
      "v_mov_b32 v189, 0\n\t"
      "v_mfma_f32_16x16x32_bf16 v[152:155], a[128:131], v[96:99], 0\n\t"
      "v_exp_f32 v53, v53\n\t"
      "v_mfma_f32_16x16x32_bf16 v[156:159], a[132:135], v[96:99], 0\n\t"
      "v_cvt_pk_bf16_f32 v80, v48, v49\n\t"
      "v_cvt_pk_bf16_f32 v179, v166, v167\n\t"
      "v_mfma_f32_16x16x32_bf16 v[144:147], a[136:139], v[84:87], v[144:147]\n\t"
      "v_exp_f32 v54, v54\n\t"
      "v_mfma_f32_16x16x32_bf16 v[148:151], a[140:143], v[84:87], v[148:151]\n\t"
      "v_cvt_pk_bf16_f32 v81, v50, v51\n\t"
      "v_cvt_pk_bf16_f32 v82, v52, v53\n\t"
      "v_mfma_f32_16x16x32_bf16 v[152:155], a[136:139], v[100:103], v[152:155]\n\t"
      "v_exp_f32 v55, v55\n\t"
      "v_mfma_f32_16x16x32_bf16 v[156:159], a[140:143], v[100:103], v[156:159]\n\t"
      "v_cvt_pk_bf16_f32 v83, v54, v55\n\t"
      "s_waitcnt vmcnt(4) lgkmcnt(0)\n\t"
      "s_barrier\n\t"
      "v_mfma_f32_16x16x32_bf16 v[144:147], a[144:147], v[88:91], v[144:147]\n\t"
      "ds_read_b128 v[204:207], v225 offset:0\n\t"
      "ds_read_b128 v[112:115], v219 offset:0\n\t"
      "v_exp_f32 v56, v56\n\t"
      "v_mfma_f32_16x16x32_bf16 v[148:151], a[148:151], v[88:91], v[148:151]\n\t"
      "ds_read_b128 v[116:119], v219 offset:1024\n\t"
      "ds_read_b128 v[120:123], v219 offset:2048\n\t"
      "v_mfma_f32_16x16x32_bf16 v[152:155], a[144:147], v[104:107], v[152:155]\n\t"
      "v_exp_f32 v57, v57\n\t"
      "v_mfma_f32_16x16x32_bf16 v[156:159], a[148:151], v[104:107], v[156:159]\n\t"
      "ds_read_b128 v[124:127], v219 offset:3072\n\t"
      "ds_read_b128 v[128:131], v219 offset:4096\n\t"
      "v_cvt_pk_bf16_f32 v96, v56, v57\n\t"
      "v_mfma_f32_16x16x32_bf16 v[144:147], a[152:155], v[92:95], v[144:147]\n\t"
      "v_exp_f32 v58, v58\n\t"
      "v_mfma_f32_16x16x32_bf16 v[148:151], a[156:159], v[92:95], v[148:151]\n\t"
      "ds_read_b128 v[132:135], v219 offset:5120\n\t"
      "ds_read_b128 v[136:139], v219 offset:6144\n\t"
      "v_mfma_f32_16x16x32_bf16 v[152:155], a[152:155], v[108:111], v[152:155]\n\t"
      "v_exp_f32 v59, v59\n\t"
      "v_mfma_f32_16x16x32_bf16 v[156:159], a[156:159], v[108:111], v[156:159]\n\t"
      "ds_read_b128 v[140:143], v219 offset:7168\n\t"
      "ds_read_b128 v[208:211], v223 offset:0\n\t"
      "v_cvt_pk_bf16_f32 v97, v58, v59\n\t"
      "v_mfma_f32_16x16x32_bf16 v[64:67], a[32:35], v[16:19], 0\n\t"
      "v_exp_f32 v60, v60\n\t"
      "v_mfma_f32_16x16x32_bf16 v[64:67], a[36:39], v[20:23], v[64:67]\n\t"
      "ds_write_b128 v218, v[80:83] offset:32768\n\t"
      "v_mfma_f32_16x16x32_bf16 v[64:67], a[40:43], v[24:27], v[64:67]\n\t"
      "v_exp_f32 v61, v61\n\t"
      "v_mfma_f32_16x16x32_bf16 v[64:67], a[44:47], v[28:31], v[64:67]\n\t"
      "v_cvt_pk_bf16_f32 v98, v60, v61\n\t"
      "s_waitcnt lgkmcnt(10)\n\t"
      "v_add_f32 v204, v204, v205\n\t"
      "v_add_f32 v206, v206, v207\n\t"
      "v_mfma_f32_16x16x32_bf16 v[68:71], a[48:51], v[16:19], 0\n\t"
      "v_exp_f32 v62, v62\n\t"
      "v_mfma_f32_16x16x32_bf16 v[68:71], a[52:55], v[20:23], v[68:71]\n\t"
      "ds_read_b128 v[212:215], v223 offset:1088\n\t"
      "v_cvt_pk_bf16_f32 v180, v168, v169\n\t"
      "v_mfma_f32_16x16x32_bf16 v[68:71], a[56:59], v[24:27], v[68:71]\n\t"
      "v_exp_f32 v63, v63\n\t"
      "v_mfma_f32_16x16x32_bf16 v[68:71], a[60:63], v[28:31], v[68:71]\n\t"
      "v_cvt_pk_bf16_f32 v99, v62, v63\n\t"
      "v_add_f32 v204, v204, v206\n\t"
      "v_mfma_f32_16x16x32_bf16 v[72:75], a[32:35], v[32:35], 0\n\t"
      "v_exp_f32 v64, v64\n\t"
      "v_mfma_f32_16x16x32_bf16 v[72:75], a[36:39], v[36:39], v[72:75]\n\t"
      "ds_write_b128 v218, v[96:99] offset:33792\n\t"
      "v_mfma_f32_16x16x32_bf16 v[72:75], a[40:43], v[40:43], v[72:75]\n\t"
      "v_exp_f32 v65, v65\n\t"
      "v_mfma_f32_16x16x32_bf16 v[72:75], a[44:47], v[44:47], v[72:75]\n\t"
      "v_cvt_pk_bf16_f32 v84, v64, v65\n\t"
      "v_rcp_f32 v197, v204\n\t"
      "v_mfma_f32_16x16x32_bf16 v[76:79], a[48:51], v[32:35], 0\n\t"
      "v_exp_f32 v66, v66\n\t"
      "v_mfma_f32_16x16x32_bf16 v[76:79], a[52:55], v[36:39], v[76:79]\n\t"
      "v_cvt_pk_bf16_f32 v176, v160, v161\n\t"
      "v_cvt_pk_bf16_f32 v181, v170, v171\n\t"
      "v_mfma_f32_16x16x32_bf16 v[76:79], a[56:59], v[40:43], v[76:79]\n\t"
      "v_exp_f32 v67, v67\n\t"
      "v_mfma_f32_16x16x32_bf16 v[76:79], a[60:63], v[44:47], v[76:79]\n\t"
      "v_cvt_pk_bf16_f32 v85, v66, v67\n\t"
      "s_waitcnt lgkmcnt(4)\n\t"
      "v_mov_b32 v198, v197\n\t"
      "v_max3_u32 v200, v200, v204, v197\n\t"
      "v_mfma_f32_16x16x32_bf16 v[144:147], a[160:163], v[112:115], v[144:147]\n\t"
      "v_exp_f32 v68, v68\n\t"
      "v_mfma_f32_16x16x32_bf16 v[148:151], a[164:167], v[112:115], v[148:151]\n\t"
      "v_cvt_pk_bf16_f32 v177, v162, v163\n\t"
      "v_cvt_pk_bf16_f32 v182, v172, v173\n\t"
      "v_mfma_f32_16x16x32_bf16 v[152:155], a[160:163], v[116:119], v[152:155]\n\t"
      "v_exp_f32 v69, v69\n\t"
      "v_mfma_f32_16x16x32_bf16 v[156:159], a[164:167], v[116:119], v[156:159]\n\t"
      "v_cvt_pk_bf16_f32 v86, v68, v69\n\t"
      "v_permlane16_swap_b32 v197, v198\n\t"
      "v_mfma_f32_16x16x32_bf16 v[144:147], a[168:171], v[120:123], v[144:147]\n\t"
      "v_exp_f32 v70, v70\n\t"
      "v_mfma_f32_16x16x32_bf16 v[148:151], a[172:175], v[120:123], v[148:151]\n\t"
      "ds_read_b128 v[112:115], v220 offset:0\n\t"
      "ds_read_b128 v[116:119], v220 offset:1024\n\t"
      "v_mfma_f32_16x16x32_bf16 v[152:155], a[168:171], v[124:127], v[152:155]\n\t"
      "v_exp_f32 v71, v71\n\t"
      "v_mfma_f32_16x16x32_bf16 v[156:159], a[172:175], v[124:127], v[156:159]\n\t"
      "v_cvt_pk_bf16_f32 v87, v70, v71\n\t"
      "v_cvt_pk_bf16_f32 v183, v174, v175\n\t"
      "v_mfma_f32_16x16x32_bf16 v[144:147], a[176:179], v[128:131], v[144:147]\n\t"
      "v_exp_f32 v72, v72\n\t"
      "s_add_u32 m0, s55, s56\n\t"
      "v_mfma_f32_16x16x32_bf16 v[148:151], a[180:183], v[128:131], v[148:151]\n\t"
      "ds_write_b128 v218, v[84:87] offset:34816\n\t"
      "v_mfma_f32_16x16x32_bf16 v[152:155], a[176:179], v[132:135], v[152:155]\n\t"
      "v_exp_f32 v73, v73\n\t"
      "v_smfmac_f32_16x16x64_bf16 v[184:187], v[192:195], v[80:87], v196\n\t"
      "v_cvt_pk_bf16_f32 v178, v164, v165\n\t"
      "v_mfma_f32_16x16x32_bf16 v[156:159], a[180:183], v[132:135], v[156:159]\n\t"
      "buffer_load_dwordx4 v226, s[36:39], s54 offen nt lds\n\t"
      "v_cvt_pk_bf16_f32 v100, v72, v73\n\t"
      "v_mfma_f32_16x16x32_bf16 v[144:147], a[184:187], v[136:139], v[144:147]\n\t"
      "v_exp_f32 v74, v74\n\t"
      "v_mfma_f32_16x16x32_bf16 v[148:151], a[188:191], v[136:139], v[148:151]\n\t"
      "ds_read_b128 v[120:123], v220 offset:2048\n\t"
      "ds_read_b128 v[124:127], v220 offset:3072\n\t"
      "v_mfma_f32_16x16x32_bf16 v[152:155], a[184:187], v[140:143], v[152:155]\n\t"
      "v_exp_f32 v75, v75\n\t"
      "v_mfma_f32_16x16x32_bf16 v[156:159], a[188:191], v[140:143], v[156:159]\n\t"
      "ds_read_b128 v[128:131], v220 offset:4096\n\t"
      "ds_read_b128 v[132:135], v220 offset:5120\n\t"
      "v_cvt_pk_bf16_f32 v101, v74, v75\n\t"
      "v_mfma_f32_16x16x32_bf16 v[48:51], a[64:67], v[16:19], 0\n\t"
      "v_exp_f32 v76, v76\n\t"
      "v_mfma_f32_16x16x32_bf16 v[48:51], a[68:71], v[20:23], v[48:51]\n\t"
      "ds_read_b128 v[136:139], v220 offset:6144\n\t"
      "ds_read_b128 v[140:143], v220 offset:7168\n\t"
      "v_mfma_f32_16x16x32_bf16 v[48:51], a[72:75], v[24:27], v[48:51]\n\t"
      "v_exp_f32 v77, v77\n\t"
      "s_add_u32 m0, s55, s57\n\t"
      "v_mfma_f32_16x16x32_bf16 v[48:51], a[76:79], v[28:31], v[48:51]\n\t"
      "buffer_load_dwordx4 v226, s[36:39], s54 offen offset:1024 nt lds\n\t"
      "v_cvt_pk_bf16_f32 v102, v76, v77\n\t"
      "v_mfma_f32_16x16x32_bf16 v[52:55], a[80:83], v[16:19], 0\n\t"
      "v_exp_f32 v78, v78\n\t"
      "v_mfma_f32_16x16x32_bf16 v[52:55], a[84:87], v[20:23], v[52:55]\n\t"
      "ds_write_b64 v222, v[176:177] offset:16384\n\t"
      "v_mfma_f32_16x16x32_bf16 v[52:55], a[88:91], v[24:27], v[52:55]\n\t"
      "v_exp_f32 v79, v79\n\t"
      "v_mfma_f32_16x16x32_bf16 v[52:55], a[92:95], v[28:31], v[52:55]\n\t"
      "v_cvt_pk_bf16_f32 v103, v78, v79\n\t"
      "s_waitcnt lgkmcnt(11)\n\t"
      "v_mfma_f32_16x16x32_bf16 v[56:59], a[64:67], v[32:35], 0\n\t"
      "v_exp_f32 v48, v48\n\t"
      "v_mfma_f32_16x16x32_bf16 v[56:59], a[68:71], v[36:39], v[56:59]\n\t"
      "ds_write_b128 v218, v[100:103] offset:35840\n\t"
      "v_mfma_f32_16x16x32_bf16 v[56:59], a[72:75], v[40:43], v[56:59]\n\t"
      "v_exp_f32 v49, v49\n\t"
      "v_smfmac_f32_16x16x64_bf16 v[188:191], v[192:195], v[96:103], v196\n\t"
      "s_add_u32 s55, s55, 8704\n\t"
      "s_cmp_eq_u32 s55, 26112\n\t"
      "s_cselect_b32 s55, 0, s55\n\t"
      "v_mfma_f32_16x16x32_bf16 v[56:59], a[76:79], v[44:47], v[56:59]\n\t"
      "buffer_store_dwordx4 v[208:211], v227, s[48:51], s58 offen sc1\n\t"
      "v_cvt_pk_bf16_f32 v88, v48, v49\n\t"
      "v_mfma_f32_16x16x32_bf16 v[60:63], a[80:83], v[32:35], 0\n\t"
      "v_exp_f32 v50, v50\n\t"
      "v_mfma_f32_16x16x32_bf16 v[60:63], a[84:87], v[36:39], v[60:63]\n\t"
      "ds_write_b64 v222, v[178:179] offset:16416\n\t"
      "v_mfma_f32_16x16x32_bf16 v[60:63], a[88:91], v[40:43], v[60:63]\n\t"
      "v_exp_f32 v51, v51\n\t"
      "v_mfma_f32_16x16x32_bf16 v[60:63], a[92:95], v[44:47], v[60:63]\n\t"
      "v_cvt_pk_bf16_f32 v89, v50, v51\n\t"
      "s_waitcnt lgkmcnt(3)\n\t"
      "v_mfma_f32_16x16x32_bf16 v[144:147], a[192:195], v[112:115], v[144:147]\n\t"
      "v_exp_f32 v52, v52\n\t"
      "v_mfma_f32_16x16x32_bf16 v[148:151], a[196:199], v[112:115], v[148:151]\n\t"
      "ds_write_b64 v222, v[180:181] offset:20736\n\t"
      "v_mfma_f32_16x16x32_bf16 v[152:155], a[192:195], v[116:119], v[152:155]\n\t"
      "v_exp_f32 v53, v53\n\t"
      "v_mfma_f32_16x16x32_bf16 v[156:159], a[196:199], v[116:119], v[156:159]\n\t"
      "buffer_store_dwordx4 v[212:215], v227, s[48:51], s58 offen offset:1024 sc1\n\t"
      "v_cvt_pk_bf16_f32 v90, v52, v53\n\t"
      "v_mfma_f32_16x16x32_bf16 v[144:147], a[200:203], v[120:123], v[144:147]\n\t"
      "v_exp_f32 v54, v54\n\t"
      "v_mfma_f32_16x16x32_bf16 v[148:151], a[204:207], v[120:123], v[148:151]\n\t"
      "ds_read_b128 v[112:115], v221 offset:0\n\t"
      "ds_read_b128 v[116:119], v221 offset:1024\n\t"
      "v_mfma_f32_16x16x32_bf16 v[152:155], a[200:203], v[124:127], v[152:155]\n\t"
      "v_exp_f32 v55, v55\n\t"
      "v_mfma_f32_16x16x32_bf16 v[156:159], a[204:207], v[124:127], v[156:159]\n\t"
      "v_cvt_pk_bf16_f32 v91, v54, v55\n\t"
      "v_add_u32 v216, s55, v217\n\t"
      "v_mfma_f32_16x16x32_bf16 v[144:147], a[208:211], v[128:131], v[144:147]\n\t"
      "v_exp_f32 v56, v56\n\t"
      "v_mfma_f32_16x16x32_bf16 v[148:151], a[212:215], v[128:131], v[148:151]\n\t"
      "ds_write_b128 v218, v[88:91] offset:36864\n\t"
      "v_mfma_f32_16x16x32_bf16 v[152:155], a[208:211], v[132:135], v[152:155]\n\t"
      "v_exp_f32 v57, v57\n\t"
      "v_mfma_f32_16x16x32_bf16 v[156:159], a[212:215], v[132:135], v[156:159]\n\t"
      "v_cvt_pk_bf16_f32 v104, v56, v57\n\t"
      "v_mfma_f32_16x16x32_bf16 v[144:147], a[216:219], v[136:139], v[144:147]\n\t"
      "v_exp_f32 v58, v58\n\t"
      "v_mfma_f32_16x16x32_bf16 v[148:151], a[220:223], v[136:139], v[148:151]\n\t"
      "ds_read_b128 v[120:123], v221 offset:2048\n\t"
      "ds_read_b128 v[124:127], v221 offset:3072\n\t"
      "v_mfma_f32_16x16x32_bf16 v[152:155], a[216:219], v[140:143], v[152:155]\n\t"
      "v_exp_f32 v59, v59\n\t"
      "v_mfma_f32_16x16x32_bf16 v[156:159], a[220:223], v[140:143], v[156:159]\n\t"
      "ds_read_b128 v[128:131], v221 offset:4096\n\t"
      "ds_read_b128 v[132:135], v221 offset:5120\n\t"
      "v_cvt_pk_bf16_f32 v105, v58, v59\n\t"
      "v_mfma_f32_16x16x32_bf16 v[64:67], a[96:99], v[16:19], 0\n\t"
      "v_exp_f32 v60, v60\n\t"
      "v_mfma_f32_16x16x32_bf16 v[64:67], a[100:103], v[20:23], v[64:67]\n\t"
      "ds_read_b128 v[136:139], v221 offset:6144\n\t"
      "ds_read_b128 v[140:143], v221 offset:7168\n\t"
      "v_mfma_f32_16x16x32_bf16 v[64:67], a[104:107], v[24:27], v[64:67]\n\t"
      "v_exp_f32 v61, v61\n\t"
      "v_mfma_f32_16x16x32_bf16 v[64:67], a[108:111], v[28:31], v[64:67]\n\t"
      "v_cvt_pk_bf16_f32 v106, v60, v61\n\t"
      "v_mfma_f32_16x16x32_bf16 v[68:71], a[112:115], v[16:19], 0\n\t"
      "v_exp_f32 v62, v62\n\t"
      "v_mfma_f32_16x16x32_bf16 v[68:71], a[116:119], v[20:23], v[68:71]\n\t"
      "ds_write_b64 v222, v[182:183] offset:20768\n\t"
      "v_mfma_f32_16x16x32_bf16 v[68:71], a[120:123], v[24:27], v[68:71]\n\t"
      "v_exp_f32 v63, v63\n\t"
      "v_mfma_f32_16x16x32_bf16 v[68:71], a[124:127], v[28:31], v[68:71]\n\t"
      "v_cvt_pk_bf16_f32 v107, v62, v63\n\t"
      "v_mfma_f32_16x16x32_bf16 v[72:75], a[96:99], v[32:35], 0\n\t"
      "v_exp_f32 v64, v64\n\t"
      "v_mfma_f32_16x16x32_bf16 v[72:75], a[100:103], v[36:39], v[72:75]\n\t"
      "ds_write_b128 v218, v[104:107] offset:37888\n\t"
      "v_mfma_f32_16x16x32_bf16 v[72:75], a[104:107], v[40:43], v[72:75]\n\t"
      "v_exp_f32 v65, v65\n\t"
      "v_mfma_f32_16x16x32_bf16 v[72:75], a[108:111], v[44:47], v[72:75]\n\t"
      "v_cvt_pk_bf16_f32 v92, v64, v65\n\t"
      "v_mfma_f32_16x16x32_bf16 v[76:79], a[112:115], v[32:35], 0\n\t"
      "v_exp_f32 v66, v66\n\t"
      "v_mfma_f32_16x16x32_bf16 v[76:79], a[116:119], v[36:39], v[76:79]\n\t"
      "ds_read_b128 v[16:19], v216 offset:0\n\t"
      "ds_read_b128 v[20:23], v216 offset:64\n\t"
      "v_mfma_f32_16x16x32_bf16 v[76:79], a[120:123], v[40:43], v[76:79]\n\t"
      "v_exp_f32 v67, v67\n\t"
      "v_mfma_f32_16x16x32_bf16 v[76:79], a[124:127], v[44:47], v[76:79]\n\t"
      "ds_read_b128 v[24:27], v216 offset:128\n\t"
      "ds_read_b128 v[28:31], v216 offset:192\n\t"
      "v_cvt_pk_bf16_f32 v93, v66, v67\n\t"
      "s_waitcnt lgkmcnt(6)\n\t"
      "v_mfma_f32_16x16x32_bf16 v[144:147], a[224:227], v[112:115], v[144:147]\n\t"
      "v_exp_f32 v68, v68\n\t"
      "v_mfma_f32_16x16x32_bf16 v[148:151], a[228:231], v[112:115], v[148:151]\n\t"
      "ds_read_b128 v[32:35], v216 offset:4352\n\t"
      "ds_read_b128 v[36:39], v216 offset:4416\n\t"
      "v_mfma_f32_16x16x32_bf16 v[152:155], a[224:227], v[116:119], v[152:155]\n\t"
      "v_exp_f32 v69, v69\n\t"
      "v_mfma_f32_16x16x32_bf16 v[156:159], a[228:231], v[116:119], v[156:159]\n\t"
      "v_cvt_pk_bf16_f32 v94, v68, v69\n\t"
      "v_mfma_f32_16x16x32_bf16 v[144:147], a[232:235], v[120:123], v[144:147]\n\t"
      "v_exp_f32 v70, v70\n\t"
      "v_mfma_f32_16x16x32_bf16 v[148:151], a[236:239], v[120:123], v[148:151]\n\t"
      "ds_read_b128 v[40:43], v216 offset:4480\n\t"
      "ds_read_b128 v[44:47], v216 offset:4544\n\t"
      "v_mfma_f32_16x16x32_bf16 v[152:155], a[232:235], v[124:127], v[152:155]\n\t"
      "v_exp_f32 v71, v71\n\t"
      "v_mfma_f32_16x16x32_bf16 v[156:159], a[236:239], v[124:127], v[156:159]\n\t"
      "v_cvt_pk_bf16_f32 v95, v70, v71\n\t"
      "v_mfma_f32_16x16x32_bf16 v[144:147], a[240:243], v[128:131], v[144:147]\n\t"
      "v_exp_f32 v72, v72\n\t"
      "v_mfma_f32_16x16x32_bf16 v[148:151], a[244:247], v[128:131], v[148:151]\n\t"
      "ds_write_b128 v218, v[92:95] offset:38912\n\t"
      "v_mfma_f32_16x16x32_bf16 v[152:155], a[240:243], v[132:135], v[152:155]\n\t"
      "v_exp_f32 v73, v73\n\t"
      "v_smfmac_f32_16x16x64_bf16 v[184:187], v[192:195], v[88:95], v196\n\t"
      "s_add_u32 s54, s54, 8192\n\t"
      "v_mfma_f32_16x16x32_bf16 v[156:159], a[244:247], v[132:135], v[156:159]\n\t"
      "v_cvt_pk_bf16_f32 v108, v72, v73\n\t"
      "v_mfma_f32_16x16x32_bf16 v[144:147], a[248:251], v[136:139], v[144:147]\n\t"
      "v_exp_f32 v74, v74\n\t"
      "v_mfma_f32_16x16x32_bf16 v[148:151], a[252:255], v[136:139], v[148:151]\n\t"
      "s_add_u32 s58, s58, 8192\n\t"
      "v_mfma_f32_16x16x32_bf16 v[152:155], a[248:251], v[140:143], v[152:155]\n\t"
      "v_exp_f32 v75, v75\n\t"
      "v_add_f32 v201, v184, v185\n\t"
      "v_mfma_f32_16x16x32_bf16 v[156:159], a[252:255], v[140:143], v[156:159]\n\t"
      "v_cvt_pk_bf16_f32 v109, v74, v75\n\t"
      "v_mov_b32 v184, 0\n\t"
      "v_mov_b32 v185, 0\n\t"
      "s_waitcnt lgkmcnt(1)\n\t"
      "s_add_u32 s53, s53, 1\n\t"
      "s_cmp_lt_u32 s53, 5\n\t"
      "s_cbranch_scc1 LOOP_%=\n\t"
      "v_exp_f32 v76, v76\n\t"
      "v_exp_f32 v77, v77\n\t"
      "v_exp_f32 v78, v78\n\t"
      "v_exp_f32 v79, v79\n\t"
      "v_mul_f32 v160, v144, v197\n\t"
      "v_mul_f32 v161, v145, v197\n\t"
      "v_mul_f32 v162, v146, v197\n\t"
      "v_mul_f32 v163, v147, v197\n\t"
      "v_cvt_pk_bf16_f32 v110, v76, v77\n\t"
      "v_cvt_pk_bf16_f32 v111, v78, v79\n\t"
      "ds_write_b128 v218, v[108:111] offset:39936\n\t"
      "v_mul_f32 v164, v148, v197\n\t"
      "v_mul_f32 v165, v149, v197\n\t"
      "v_mul_f32 v166, v150, v197\n\t"
      "v_mul_f32 v167, v151, v197\n\t"
      "v_mul_f32 v168, v152, v198\n\t"
      "v_mul_f32 v169, v153, v198\n\t"
      "v_mul_f32 v170, v154, v198\n\t"
      "v_mul_f32 v171, v155, v198\n\t"
      "v_mul_f32 v172, v156, v198\n\t"
      "v_mul_f32 v173, v157, v198\n\t"
      "v_mul_f32 v174, v158, v198\n\t"
      "v_mul_f32 v175, v159, v198\n\t"
      "v_mfma_f32_16x16x32_bf16 v[144:147], a[128:131], v[80:83], 0\n\t"
      "v_smfmac_f32_16x16x64_bf16 v[188:191], v[192:195], v[104:111], v196\n\t"
      "v_mfma_f32_16x16x32_bf16 v[148:151], a[132:135], v[80:83], 0\n\t"
      "v_cvt_pk_bf16_f32 v176, v160, v161\n\t"
      "v_cvt_pk_bf16_f32 v177, v162, v163\n\t"
      "v_mfma_f32_16x16x32_bf16 v[152:155], a[128:131], v[96:99], 0\n\t"
      "v_cvt_pk_bf16_f32 v178, v164, v165\n\t"
      "v_cvt_pk_bf16_f32 v179, v166, v167\n\t"
      "v_mfma_f32_16x16x32_bf16 v[156:159], a[132:135], v[96:99], 0\n\t"
      "ds_write_b64 v222, v[176:177] offset:0\n\t"
      "v_cvt_pk_bf16_f32 v180, v168, v169\n\t"
      "v_cvt_pk_bf16_f32 v181, v170, v171\n\t"
      "v_add_f32 v202, v188, v189\n\t"
      "v_cndmask_b32 v201, v201, v202, s[60:61]\n\t"
      "v_mfma_f32_16x16x32_bf16 v[144:147], a[136:139], v[84:87], v[144:147]\n\t"
      "ds_write_b32 v224, v201 offset:512\n\t"
      "v_cvt_pk_bf16_f32 v182, v172, v173\n\t"
      "v_cvt_pk_bf16_f32 v183, v174, v175\n\t"
      "v_mfma_f32_16x16x32_bf16 v[148:151], a[140:143], v[84:87], v[148:151]\n\t"
      "ds_write_b64 v222, v[178:179] offset:32\n\t"
      "v_mfma_f32_16x16x32_bf16 v[152:155], a[136:139], v[100:103], v[152:155]\n\t"
      "ds_write_b64 v222, v[180:181] offset:4352\n\t"
      "v_mfma_f32_16x16x32_bf16 v[156:159], a[140:143], v[100:103], v[156:159]\n\t"
      "ds_write_b64 v222, v[182:183] offset:4384\n\t"
      "v_mfma_f32_16x16x32_bf16 v[144:147], a[144:147], v[88:91], v[144:147]\n\t"
      "v_mfma_f32_16x16x32_bf16 v[148:151], a[148:151], v[88:91], v[148:151]\n\t"
      "s_waitcnt lgkmcnt(0)\n\t"
      "s_barrier\n\t"
      "v_mfma_f32_16x16x32_bf16 v[152:155], a[144:147], v[104:107], v[152:155]\n\t"
      "ds_read_b128 v[112:115], v219 offset:32768\n\t"
      "ds_read_b128 v[116:119], v219 offset:33792\n\t"
      "v_mfma_f32_16x16x32_bf16 v[156:159], a[148:151], v[104:107], v[156:159]\n\t"
      "ds_read_b128 v[120:123], v219 offset:34816\n\t"
      "ds_read_b128 v[124:127], v219 offset:35840\n\t"
      "v_mfma_f32_16x16x32_bf16 v[144:147], a[152:155], v[92:95], v[144:147]\n\t"
      "ds_read_b128 v[128:131], v219 offset:36864\n\t"
      "ds_read_b128 v[132:135], v219 offset:37888\n\t"
      "v_mfma_f32_16x16x32_bf16 v[148:151], a[156:159], v[92:95], v[148:151]\n\t"
      "ds_read_b128 v[136:139], v219 offset:38912\n\t"
      "ds_read_b128 v[140:143], v219 offset:39936\n\t"
      "v_mfma_f32_16x16x32_bf16 v[152:155], a[152:155], v[108:111], v[152:155]\n\t"
      "ds_read_b128 v[204:207], v225 offset:512\n\t"
      "ds_read_b128 v[16:19], v220 offset:32768\n\t"
      "v_mfma_f32_16x16x32_bf16 v[156:159], a[156:159], v[108:111], v[156:159]\n\t"
      "s_waitcnt lgkmcnt(2)\n\t"
      "v_mfma_f32_16x16x32_bf16 v[144:147], a[160:163], v[112:115], v[144:147]\n\t"
      "ds_read_b128 v[208:211], v223 offset:16384\n\t"
      "ds_read_b128 v[212:215], v223 offset:17472\n\t"
      "v_mfma_f32_16x16x32_bf16 v[148:151], a[164:167], v[112:115], v[148:151]\n\t"
      "ds_read_b128 v[160:163], v223 offset:0\n\t"
      "ds_read_b128 v[164:167], v223 offset:1088\n\t"
      "v_mfma_f32_16x16x32_bf16 v[152:155], a[160:163], v[116:119], v[152:155]\n\t"
      "ds_read_b128 v[20:23], v220 offset:33792\n\t"
      "ds_read_b128 v[24:27], v220 offset:34816\n\t"
      "v_mfma_f32_16x16x32_bf16 v[156:159], a[164:167], v[116:119], v[156:159]\n\t"
      "ds_read_b128 v[28:31], v220 offset:35840\n\t"
      "ds_read_b128 v[32:35], v220 offset:36864\n\t"
      "s_waitcnt lgkmcnt(9)\n\t"
      "v_add_f32 v204, v204, v205\n\t"
      "v_add_f32 v206, v206, v207\n\t"
      "v_mfma_f32_16x16x32_bf16 v[144:147], a[168:171], v[120:123], v[144:147]\n\t"
      "ds_read_b128 v[36:39], v220 offset:37888\n\t"
      "ds_read_b128 v[40:43], v220 offset:38912\n\t"
      "v_mfma_f32_16x16x32_bf16 v[148:151], a[172:175], v[120:123], v[148:151]\n\t"
      "ds_read_b128 v[44:47], v220 offset:39936\n\t"
      "v_add_f32 v204, v204, v206\n\t"
      "v_mfma_f32_16x16x32_bf16 v[152:155], a[168:171], v[124:127], v[152:155]\n\t"
      "ds_read_b128 v[48:51], v221 offset:32768\n\t"
      "ds_read_b128 v[52:55], v221 offset:33792\n\t"
      "s_waitcnt lgkmcnt(9)\n\t"
      "v_mfma_f32_16x16x32_bf16 v[156:159], a[172:175], v[124:127], v[156:159]\n\t"
      "ds_read_b128 v[56:59], v221 offset:34816\n\t"
      "ds_read_b128 v[60:63], v221 offset:35840\n\t"
      "buffer_store_dwordx4 v[208:211], v227, s[48:51], s58 offen sc1\n\t"
      "v_rcp_f32 v197, v204\n\t"
      "v_mfma_f32_16x16x32_bf16 v[144:147], a[176:179], v[128:131], v[144:147]\n\t"
      "ds_read_b128 v[64:67], v221 offset:36864\n\t"
      "ds_read_b128 v[68:71], v221 offset:37888\n\t"
      "v_mfma_f32_16x16x32_bf16 v[148:151], a[180:183], v[128:131], v[148:151]\n\t"
      "ds_read_b128 v[72:75], v221 offset:38912\n\t"
      "ds_read_b128 v[76:79], v221 offset:39936\n\t"
      "buffer_store_dwordx4 v[212:215], v227, s[48:51], s58 offen offset:1024 sc1\n\t"
      "v_mov_b32 v198, v197\n\t"
      "v_max3_u32 v200, v200, v204, v197\n\t"
      "v_mfma_f32_16x16x32_bf16 v[152:155], a[176:179], v[132:135], v[152:155]\n\t"
      "s_add_u32 s58, s58, 8192\n\t"
      "v_mfma_f32_16x16x32_bf16 v[156:159], a[180:183], v[132:135], v[156:159]\n\t"
      "buffer_store_dwordx4 v[160:163], v227, s[48:51], s58 offen sc1\n\t"
      "v_mfma_f32_16x16x32_bf16 v[144:147], a[184:187], v[136:139], v[144:147]\n\t"
      "v_mfma_f32_16x16x32_bf16 v[148:151], a[188:191], v[136:139], v[148:151]\n\t"
      "buffer_store_dwordx4 v[164:167], v227, s[48:51], s58 offen offset:1024 sc1\n\t"
      "v_permlane16_swap_b32 v197, v198\n\t"
      "v_mfma_f32_16x16x32_bf16 v[152:155], a[184:187], v[140:143], v[152:155]\n\t"
      "s_add_u32 s58, s58, 8192\n\t"
      "v_mfma_f32_16x16x32_bf16 v[156:159], a[188:191], v[140:143], v[156:159]\n\t"
      "s_waitcnt lgkmcnt(8)\n\t"
      "v_mfma_f32_16x16x32_bf16 v[144:147], a[192:195], v[16:19], v[144:147]\n\t"
      "v_mfma_f32_16x16x32_bf16 v[148:151], a[196:199], v[16:19], v[148:151]\n\t"
      "v_mfma_f32_16x16x32_bf16 v[152:155], a[192:195], v[20:23], v[152:155]\n\t"
      "v_mfma_f32_16x16x32_bf16 v[156:159], a[196:199], v[20:23], v[156:159]\n\t"
      "v_mfma_f32_16x16x32_bf16 v[144:147], a[200:203], v[24:27], v[144:147]\n\t"
      "v_mfma_f32_16x16x32_bf16 v[148:151], a[204:207], v[24:27], v[148:151]\n\t"
      "v_mfma_f32_16x16x32_bf16 v[152:155], a[200:203], v[28:31], v[152:155]\n\t"
      "v_mfma_f32_16x16x32_bf16 v[156:159], a[204:207], v[28:31], v[156:159]\n\t"
      "v_mfma_f32_16x16x32_bf16 v[144:147], a[208:211], v[32:35], v[144:147]\n\t"
      "v_mfma_f32_16x16x32_bf16 v[148:151], a[212:215], v[32:35], v[148:151]\n\t"
      "v_mfma_f32_16x16x32_bf16 v[152:155], a[208:211], v[36:39], v[152:155]\n\t"
      "v_mfma_f32_16x16x32_bf16 v[156:159], a[212:215], v[36:39], v[156:159]\n\t"
      "v_mfma_f32_16x16x32_bf16 v[144:147], a[216:219], v[40:43], v[144:147]\n\t"
      "v_mfma_f32_16x16x32_bf16 v[148:151], a[220:223], v[40:43], v[148:151]\n\t"
      "s_add_u32 s62, s58, 4096\n\t"
      "v_mfma_f32_16x16x32_bf16 v[152:155], a[216:219], v[44:47], v[152:155]\n\t"
      "v_mbcnt_lo_u32_b32 v239, -1, 0\n\t"
      "v_mbcnt_hi_u32_b32 v239, -1, v239\n\t"
      "v_mfma_f32_16x16x32_bf16 v[156:159], a[220:223], v[44:47], v[156:159]\n\t"
      "s_waitcnt lgkmcnt(0)\n\t"
      "v_mfma_f32_16x16x32_bf16 v[144:147], a[224:227], v[48:51], v[144:147]\n\t"
      "v_and_b32 v240, 15, v239\n\t"
      "v_lshrrev_b32 v239, 4, v239\n\t"
      "v_mfma_f32_16x16x32_bf16 v[148:151], a[228:231], v[48:51], v[148:151]\n\t"
      "v_mfma_f32_16x16x32_bf16 v[152:155], a[224:227], v[52:55], v[152:155]\n\t"
      "v_lshlrev_b32 v240, 8, v240\n\t"
      "v_lshlrev_b32 v239, 3, v239\n\t"
      "v_mfma_f32_16x16x32_bf16 v[156:159], a[228:231], v[52:55], v[156:159]\n\t"
      "v_mfma_f32_16x16x32_bf16 v[144:147], a[232:235], v[56:59], v[144:147]\n\t"
      "v_add_u32 v239, v239, v240\n\t"
      "s_lshl_b32 s63, s52, 6\n\t"
      "v_mfma_f32_16x16x32_bf16 v[148:151], a[236:239], v[56:59], v[148:151]\n\t"
      "v_mfma_f32_16x16x32_bf16 v[152:155], a[232:235], v[60:63], v[152:155]\n\t"
      "v_add_u32 v239, s63, v239\n\t"
      "v_mfma_f32_16x16x32_bf16 v[156:159], a[236:239], v[60:63], v[156:159]\n\t"
      "v_mfma_f32_16x16x32_bf16 v[144:147], a[240:243], v[64:67], v[144:147]\n\t"
      "v_mfma_f32_16x16x32_bf16 v[148:151], a[244:247], v[64:67], v[148:151]\n\t"
      "v_mfma_f32_16x16x32_bf16 v[152:155], a[240:243], v[68:71], v[152:155]\n\t"
      "v_mfma_f32_16x16x32_bf16 v[156:159], a[244:247], v[68:71], v[156:159]\n\t"
      "v_mfma_f32_16x16x32_bf16 v[144:147], a[248:251], v[72:75], v[144:147]\n\t"
      "v_mfma_f32_16x16x32_bf16 v[148:151], a[252:255], v[72:75], v[148:151]\n\t"
      "v_mfma_f32_16x16x32_bf16 v[152:155], a[248:251], v[76:79], v[152:155]\n\t"
      "v_mfma_f32_16x16x32_bf16 v[156:159], a[252:255], v[76:79], v[156:159]\n\t"
      "s_nop 3\n\t"
      "s_nop 0\n\t"
      "v_mul_f32 v160, v144, v197\n\t"
      "v_mul_f32 v161, v145, v197\n\t"
      "v_mul_f32 v162, v146, v197\n\t"
      "v_mul_f32 v163, v147, v197\n\t"
      "v_cvt_pk_bf16_f32 v176, v160, v161\n\t"
      "v_cvt_pk_bf16_f32 v177, v162, v163\n\t"
      "s_nop 0\n\t"
      "v_mul_f32 v164, v148, v197\n\t"
      "v_mul_f32 v165, v149, v197\n\t"
      "v_mul_f32 v166, v150, v197\n\t"
      "v_mul_f32 v167, v151, v197\n\t"
      "v_cvt_pk_bf16_f32 v178, v164, v165\n\t"
      "v_cvt_pk_bf16_f32 v179, v166, v167\n\t"
      "s_nop 0\n\t"
      "v_mul_f32 v168, v152, v198\n\t"
      "v_mul_f32 v169, v153, v198\n\t"
      "v_mul_f32 v170, v154, v198\n\t"
      "v_mul_f32 v171, v155, v198\n\t"
      "v_cvt_pk_bf16_f32 v180, v168, v169\n\t"
      "v_cvt_pk_bf16_f32 v181, v170, v171\n\t"
      "v_mul_f32 v172, v156, v198\n\t"
      "v_mul_f32 v173, v157, v198\n\t"
      "v_mul_f32 v174, v158, v198\n\t"
      "v_mul_f32 v175, v159, v198\n\t"
      "v_cvt_pk_bf16_f32 v182, v172, v173\n\t"
      "v_cvt_pk_bf16_f32 v183, v174, v175\n\t"
      "buffer_store_dwordx2 v[176:177], v239, s[48:51], s58 offen\n\t"
      "buffer_store_dwordx2 v[178:179], v239, s[48:51], s58 offen offset:32\n\t"
      "buffer_store_dwordx2 v[180:181], v239, s[48:51], s62 offen\n\t"
      "buffer_store_dwordx2 v[182:183], v239, s[48:51], s62 offen offset:32\n\t"
      "s_mov_b32 s63, 0x53800000\n\t"
      "v_cmp_lt_u32 vcc, s63, v200\n\t"
      "s_nop 3\n\t"
      "s_cmp_lg_u64 vcc, 0\n\t"
      "s_cselect_b32 %[bad], 1, 0\n\t"
      : [bad] "=&s"(bad)
      : [qb] "s"(qb), [kb] "s"(kb), [vb] "s"(vb), [ob] "s"(ob), [wid] "s"(wid), [hq] "s"(hq)
      : "v0", "v1", "v2", "v3", "v4", "v5", "v6", "v7", "v8", "v9", "v10", "v11", "v12", "v13", "v14", "v15", "v16", "v17", "v18", "v19", "v20", "v21", "v22", "v23", "v24", "v25", "v26", "v27", "v28", "v29", "v30", "v31", "v32", "v33", "v34", "v35", "v36", "v37", "v38", "v39", "v40", "v41", "v42", "v43", "v44", "v45", "v46", "v47", "v48", "v49", "v50", "v51", "v52", "v53", "v54", "v55", "v56", "v57", "v58", "v59", "v60", "v61", "v62", "v63", "v64", "v65", "v66", "v67", "v68", "v69", "v70", "v71", "v72", "v73", "v74", "v75", "v76", "v77", "v78", "v79", "v80", "v81", "v82", "v83", "v84", "v85", "v86", "v87", "v88", "v89", "v90", "v91", "v92", "v93", "v94", "v95", "v96", "v97", "v98", "v99", "v100", "v101", "v102", "v103", "v104", "v105", "v106", "v107", "v108", "v109", "v110", "v111", "v112", "v113", "v114", "v115", "v116", "v117", "v118", "v119", "v120", "v121", "v122", "v123", "v124", "v125", "v126", "v127", "v128", "v129", "v130", "v131", "v132", "v133", "v134", "v135", "v136", "v137", "v138", "v139", "v140", "v141", "v142", "v143", "v144", "v145", "v146", "v147", "v148", "v149", "v150", "v151", "v152", "v153", "v154", "v155", "v156", "v157", "v158", "v159", "v160", "v161", "v162", "v163", "v164", "v165", "v166", "v167", "v168", "v169", "v170", "v171", "v172", "v173", "v174", "v175", "v176", "v177", "v178", "v179", "v180", "v181", "v182", "v183", "v184", "v185", "v186", "v187", "v188", "v189", "v190", "v191", "v192", "v193", "v194", "v195", "v196", "v197", "v198", "v199", "v200", "v201", "v202", "v203", "v204", "v205", "v206", "v207", "v208", "v209", "v210", "v211", "v212", "v213", "v214", "v215", "v216", "v217", "v218", "v219", "v220", "v221", "v222", "v223", "v224", "v225", "v226", "v227", "v228", "v229", "v230", "v231", "v232", "v233", "v234", "v235", "v236", "v237", "v238", "v239", "v240", "v241", "v242", "v243", "v244", "v245", "v246", "v247", "v248", "v249", "v250", "v251", "v252", "v253", "v254", "v255", "a0", "a1", "a2", "a3", "a4", "a5", "a6", "a7", "a8", "a9", "a10", "a11", "a12", "a13", "a14", "a15", "a16", "a17", "a18", "a19", "a20", "a21", "a22", "a23", "a24", "a25", "a26", "a27", "a28", "a29", "a30", "a31", "a32", "a33", "a34", "a35", "a36", "a37", "a38", "a39", "a40", "a41", "a42", "a43", "a44", "a45", "a46", "a47", "a48", "a49", "a50", "a51", "a52", "a53", "a54", "a55", "a56", "a57", "a58", "a59", "a60", "a61", "a62", "a63", "a64", "a65", "a66", "a67", "a68", "a69", "a70", "a71", "a72", "a73", "a74", "a75", "a76", "a77", "a78", "a79", "a80", "a81", "a82", "a83", "a84", "a85", "a86", "a87", "a88", "a89", "a90", "a91", "a92", "a93", "a94", "a95", "a96", "a97", "a98", "a99", "a100", "a101", "a102", "a103", "a104", "a105", "a106", "a107", "a108", "a109", "a110", "a111", "a112", "a113", "a114", "a115", "a116", "a117", "a118", "a119", "a120", "a121", "a122", "a123", "a124", "a125", "a126", "a127", "a128", "a129", "a130", "a131", "a132", "a133", "a134", "a135", "a136", "a137", "a138", "a139", "a140", "a141", "a142", "a143", "a144", "a145", "a146", "a147", "a148", "a149", "a150", "a151", "a152", "a153", "a154", "a155", "a156", "a157", "a158", "a159", "a160", "a161", "a162", "a163", "a164", "a165", "a166", "a167", "a168", "a169", "a170", "a171", "a172", "a173", "a174", "a175", "a176", "a177", "a178", "a179", "a180", "a181", "a182", "a183", "a184", "a185", "a186", "a187", "a188", "a189", "a190", "a191", "a192", "a193", "a194", "a195", "a196", "a197", "a198", "a199", "a200", "a201", "a202", "a203", "a204", "a205", "a206", "a207", "a208", "a209", "a210", "a211", "a212", "a213", "a214", "a215", "a216", "a217", "a218", "a219", "a220", "a221", "a222", "a223", "a224", "a225", "a226", "a227", "a228", "a229", "a230", "a231", "a232", "a233", "a234", "a235", "a236", "a237", "a238", "a239", "a240", "a241", "a242", "a243", "a244", "a245", "a246", "a247", "a248", "a249", "a250", "a251", "a252", "a253", "a254", "a255", "s36", "s37", "s38", "s39", "s40", "s41", "s42", "s43", "s44", "s45", "s46", "s47", "s48", "s49", "s50", "s51", "s52", "s53", "s54", "s55", "s56", "s57", "s58", "s59", "s60", "s61", "s62", "s63", "s64", "s65", "s66", "s67", "s68", "s69", "s70", "s71", "s72", "s73", "s74", "s75", "s76", "s77", "s78", "s79", "s80", "s81", "s82", "s83", "s84", "s85", "s86", "s87", "s88", "s89", "s90", "s91", "s92", "s93", "s94", "s95", "s96", "s97", "s98", "s99", "vcc", "scc", "memory");
  if (bad) {
    asm volatile("s_waitcnt vmcnt(0)" ::: "memory");
    __syncthreads();
    const u32 lane = __builtin_amdgcn_mbcnt_hi(~0u, __builtin_amdgcn_mbcnt_lo(~0u, 0u));
    float* qs = (float*)lds + wid * 128u;
    float* ps = (float*)(lds + 4096) + wid * 512u;
    const u16* qh = (const u16*)qb; const u16* kh = (const u16*)kb; const u16* vh = (const u16*)vb; u16* oh = (u16*)ob;
    for (u32 row = wid; row < 384u; row += 4u) {
      const u32 qq = *(const u32*)(qh + row * 128u + lane * 2u);
      qs[lane * 2u] = bf_lo(qq); qs[lane * 2u + 1u] = bf_hi(qq);
      __syncthreads();
      float s[8]; float m = -INFINITY;
#pragma unroll
      for (int i = 0; i < 8; ++i) {
        const u16* kr = kh + (i * 64u + lane) * 128u;
        float a = 0.f;
        for (int c = 0; c < 16; ++c) {
          const u32x4 x = *(const u32x4*)(kr + c * 8);
#pragma unroll
          for (int e = 0; e < 4; ++e) { a = fmaf(qs[c * 8 + 2 * e], bf_lo(x[e]), a); a = fmaf(qs[c * 8 + 2 * e + 1], bf_hi(x[e]), a); }
        }
        s[i] = a * 0.08838834764831845f; m = fmaxf(m, s[i]);
      }
      for (int d = 32; d > 0; d >>= 1) m = fmaxf(m, __shfl_xor(m, d));
      float l = 0.f;
#pragma unroll
      for (int i = 0; i < 8; ++i) { const float p = __expf(s[i] - m); l += p; ps[i * 64u + lane] = p; }
      for (int d = 32; d > 0; d >>= 1) l += __shfl_xor(l, d);
      __syncthreads();
      float a0 = 0.f, a1 = 0.f;
      for (u32 key = 0; key < 512u; ++key) { const float p = ps[key]; const u32 x = *(const u32*)(vh + key * 128u + lane * 2u); a0 = fmaf(p, bf_lo(x), a0); a1 = fmaf(p, bf_hi(x), a1); }
      const float r = 1.f / l;
      *(u32*)(oh + row * 128u + lane * 2u) = (u32)f2bf(a0 * r) | ((u32)f2bf(a1 * r) << 16);
      __syncthreads();
    }
  }
}

extern "C" void kernel_launch(void* const* d_in, const int* in_sizes, int n_in, void* d_out, int out_size, void* d_ws, size_t ws_size,
                              hipStream_t stream) {
  (void)in_sizes; (void)n_in; (void)out_size; (void)ws_size; (void)d_ws;
  hipLaunchKernelGGL(standalone__gqa_softmax__near_ridge_kernel, dim3(256), dim3(256), 0, stream, (const u16*)d_in[0], (const u16*)d_in[1], (const u16*)d_in[2], (u16*)d_out);
}
```
